```python
import torch

def _softmax(x, axis=-1):
    return torch.softmax(x, dim=axis)
def _gelu(x):
    return torch.nn.functional.gelu(x, approximate="tanh")
def _silu(x):
    return torch.nn.functional.silu(x)
def _sigmoid(x):
    return torch.sigmoid(x)
def _layernorm(x, axis=-1, eps=1e-5):
    m = x.mean(dim=axis, keepdim=True)
    v = x.var(dim=axis, keepdim=True, unbiased=False)
    return (x - m) / torch.sqrt(v + eps)
def _scatter_add(tgt, idx, src, axis=0):
    return tgt.clone().index_add_(axis, idx, src)
def _segment_sum(src, seg, n):
    out = torch.zeros((n,) + src.shape[1:], dtype=src.dtype)
    return out.index_add_(0, seg, src)
def _pad(x, pw):
    flat = tuple(v for pair in reversed(pw) for v in pair)
    return torch.nn.functional.pad(x, flat)

kernel_name = 'sequential_preLN__sliding_block_cached__swiglu_ffn__decode'

def setup_inputs():

    _w = (lambda a: (lambda t: t.to(torch.bfloat16) if t.is_floating_point() else t)(torch.as_tensor(a)))
    def _A_setup():
        B, Sq, D = 4, 1, 512
        Hq, Hkv, Sk, Dh = 32, 32, 8192, 128
        assert Hq % Hkv == 0, (Hq, Hkv)
        return {
            "x": _w((lambda *s: torch.randn(*s, dtype=torch.bfloat16) * 0.05)(B, Sq, D)),
            "k_cache": _w((lambda *s: torch.randn(*s, dtype=torch.bfloat16) * 3.2)(B, Hkv, Sk, Dh)),
            "v_cache": _w((lambda *s: torch.randn(*s, dtype=torch.bfloat16) * 0.4)(B, Hkv, Sk, Dh)),
            "wq": _w((lambda *s: torch.randn(*s, dtype=torch.bfloat16) * 3.2)(D, Hq * Dh) * (D ** -0.5)),
            "wo": _w((lambda *s: torch.randn(*s, dtype=torch.bfloat16) * 0.4)(Hq * Dh, D) * ((Hq * Dh) ** -0.5)),
        }
    _A_in = _A_setup()

    def _B_setup():
        B, S, D, F = 4, 1, 512, 1376
        return {
            "x": _w((lambda *s: torch.randn(*s, dtype=torch.bfloat16) * 0.05)(B, S, D)),
            "w_gate": _w((lambda *s: torch.randn(*s, dtype=torch.bfloat16) * 0.05)(D, F)),
            "w_up": _w((lambda *s: torch.randn(*s, dtype=torch.bfloat16) * 0.05)(D, F)),
            "w_down": _w((lambda *s: torch.randn(*s, dtype=torch.bfloat16) * 0.05)(F, D)),
        }
    _B_in = _B_setup()

    return {
        "A_x": _A_in["x"],
        "A_k_cache": _A_in["k_cache"],
        "A_v_cache": _A_in["v_cache"],
        "A_wq": _A_in["wq"],
        "A_wo": _A_in["wo"],
        "B_x": _B_in["x"],
        "B_w_gate": _B_in["w_gate"],
        "B_w_up": _B_in["w_up"],
        "B_w_down": _B_in["w_down"],
    }

def reference(A_x, A_k_cache, A_v_cache, A_wq, A_wo, B_x, B_w_gate, B_w_up, B_w_down):
    def _A_ref(x, k_cache, v_cache, wq, wo):
        B, Sq, D = x.shape
        Hq, Dh = 32, 128
        Hkv, Sk = k_cache.shape[1], k_cache.shape[2]
        W = min(4096, max(256, Sk // 4))
        rep = Hq // Hkv
        q = (lambda x, *d: x.permute(*d))((x @ wq).reshape(B, Sq, Hq, Dh), 0, 2, 1, 3)
        k = (lambda x, n, axis: torch.repeat_interleave(x, n, dim=axis))(k_cache[:, :, Sk - W:, :], rep, axis=1)
        v = (lambda x, n, axis: torch.repeat_interleave(x, n, dim=axis))(v_cache[:, :, Sk - W:, :], rep, axis=1)
        scores = torch.einsum("bhqd,bhkd->bhqk", q, k) * (Dh ** -0.5)
        p = _softmax(scores, axis=-1)
        o = torch.einsum("bhqk,bhkd->bhqd", p, v)
        return (lambda x, *d: x.permute(*d))(o, 0, 2, 1, 3).reshape(B, Sq, Hq * Dh) @ wo

    def _B_ref(x, w_gate, w_up, w_down):
        g = _silu(x @ w_gate)
        u = x @ w_up
        return (g * u) @ w_down

    x = A_x
    h = x + _A_ref(_layernorm(x, axis=-1), A_k_cache, A_v_cache, A_wq, A_wo)
    h2 = h + _B_ref(_layernorm(h, axis=-1), B_w_gate, B_w_up, B_w_down)
    return h2
```

```cpp
#include <hip/hip_runtime.h>
#include <hip/hip_bf16.h>
#include <stdint.h>
#define MIMIC 1
typedef unsigned short u16;
typedef unsigned u32x4 __attribute__((ext_vector_type(4)));
__device__ __forceinline__ float bf2f(u16 b){ return __builtin_bit_cast(float, (unsigned)b<<16); }
__device__ __forceinline__ u16 f2bf(float f){ __hip_bfloat16 h = __float2bfloat16(f); return __builtin_bit_cast(u16,h); }
__device__ __forceinline__ float rb(float f){
  return bf2f(f2bf(f));
}
constexpr int B=4, D=512, H=32, SK=8192, DH=128, W=2048, F=1376;

__device__ float block_sum(float v, float* red){
  for(int o=32;o>0;o>>=1) v += __shfl_xor(v,o);
  int w=threadIdx.x>>6;
  __syncthreads();
  if((threadIdx.x&63)==0) red[w]=v;
  __syncthreads();
  float s=0; for(int i=0;i<(int)(blockDim.x>>6);++i) s+=red[i];
  return s;
}
__device__ void layernorm512(const float* xs, float* xn, float* red){
  int t=threadIdx.x;
  float a=xs[t], b=xs[t+256];
  float mean = block_sum(a+b, red)/512.f;
  float da=a-mean, db=b-mean;
  float var = block_sum(da*da+db*db, red)/512.f;
  float m=rb(mean), v=rb(var);
  float sd = rb(sqrtf(rb(v+1e-5f)));
  xn[t] = rb(rb(a-m)/sd); xn[t+256]=rb(rb(b-m)/sd);
  __syncthreads();
}
__global__ void __launch_bounds__(256) k_q(const u16* x, const u16* wq, float* q){
  __shared__ float xs[512], xn[512], red[8], part[2][128];
  int h=blockIdx.x,b=blockIdx.y,t=threadIdx.x;
  xs[t]=bf2f(x[b*512+t]); xs[t+256]=bf2f(x[b*512+t+256]);
  __syncthreads();
  layernorm512(xs,xn,red);
  int d=t&127, half=t>>7; float acc=0;
  for(int k=half*256;k<half*256+256;++k) acc += xn[k]*bf2f(wq[(size_t)k*4096+h*128+d]);
  part[half][d]=acc; __syncthreads();
  if(t<128) q[(b*32+h)*128+t]=rb(part[0][t]+part[1][t]);
}
__global__ void __launch_bounds__(256) k_attn(const float* q, const u16* kc, const u16* vc, float* o){
  __shared__ float sc[W], qs[128], red[8], op[2][128];
  int bh=blockIdx.x,t=threadIdx.x;
  if(t<128) qs[t]=q[bh*128+t];
  __syncthreads();
  const u16* kb = kc + ((size_t)bh*SK + (SK-W))*DH;
  const u16* vb = vc + ((size_t)bh*SK + (SK-W))*DH;
  float mx=-INFINITY;
  for(int j=t;j<W;j+=256){
    float a=0; const u32x4* kr=(const u32x4*)(kb+(size_t)j*DH);
    for(int c=0;c<16;++c){ u32x4 v=kr[c];
      for(int e=0;e<4;++e){ a+=qs[c*8+2*e]*__builtin_bit_cast(float,v[e]<<16); a+=qs[c*8+2*e+1]*__builtin_bit_cast(float,v[e]&0xffff0000u);} }
    float s=rb(rb(a)*0.08838834764831845f);
    sc[j]=s; mx=fmaxf(mx,s);
  }
  for(int of=32;of>0;of>>=1) mx=fmaxf(mx,__shfl_xor(mx,of));
  __syncthreads(); if((t&63)==0) red[t>>6]=mx; __syncthreads();
  mx=fmaxf(fmaxf(red[0],red[1]),fmaxf(red[2],red[3]));
  float sum=0; for(int j=t;j<W;j+=256){ float e=__expf(sc[j]-mx); sc[j]=e; sum+=e; }
  sum=block_sum(sum,red);
  float inv=1.f/sum;
  for(int j=t;j<W;j+=256) sc[j]=rb(sc[j]*inv);
  __syncthreads();
  int d=t&127, half=t>>7; float acc=0;
  for(int j=half*1024;j<half*1024+1024;++j) acc+=sc[j]*bf2f(vb[(size_t)j*DH+d]);
  op[half][d]=acc; __syncthreads();
  if(t<128) o[bh*128+t]=rb(op[0][t]+op[1][t]);
}
__global__ void __launch_bounds__(256) k_wo(const float* o, const u16* wo, const u16* x, float* h){
  __shared__ float part[8][32];
  int b=blockIdx.y,c=blockIdx.x*32+(threadIdx.x&31),g=threadIdx.x>>5;
  float acc=0; for(int k=g*512;k<g*512+512;++k) acc+=o[b*4096+k]*bf2f(wo[(size_t)k*512+c]);
  part[g][threadIdx.x&31]=acc; __syncthreads();
  if(threadIdx.x<32){ float s=0; for(int i=0;i<8;++i) s+=part[i][threadIdx.x]; h[b*512+c]=rb(bf2f(x[b*512+c])+rb(s)); }
}
__global__ void __launch_bounds__(256) k_ffn1(const float* h, const u16* wg, const u16* wu, float* act){
  __shared__ float xs[512], xn[512], red[8], pg[8][32], pu[8][32];
  int b=blockIdx.y,t=threadIdx.x;
  xs[t]=h[b*512+t]; xs[t+256]=h[b*512+t+256]; __syncthreads();
  layernorm512(xs,xn,red);
  int c=blockIdx.x*32+(t&31), g=t>>5; float ag=0,au=0;
  for(int k=g*64;k<g*64+64;++k){ ag+=xn[k]*bf2f(wg[(size_t)k*F+c]); au+=xn[k]*bf2f(wu[(size_t)k*F+c]); }
  pg[g][t&31]=ag; pu[g][t&31]=au; __syncthreads();
  if(t<32){ float sg=0,su=0; for(int i=0;i<8;++i){sg+=pg[i][t]; su+=pu[i][t];}
    sg=rb(sg); su=rb(su); float si=rb(sg/(1.f+__expf(-sg))); act[b*F+c]=rb(si*su); }
}
__global__ void __launch_bounds__(256) k_ffn2(const float* act, const u16* wd, const float* h, u16* out){
  __shared__ float part[8][32];
  int b=blockIdx.y,c=blockIdx.x*32+(threadIdx.x&31),g=threadIdx.x>>5;
  float acc=0; for(int k=g*172;k<g*172+172;++k) acc+=act[b*F+k]*bf2f(wd[(size_t)k*512+c]);
  part[g][threadIdx.x&31]=acc; __syncthreads();
  if(threadIdx.x<32){ float s=0; for(int i=0;i<8;++i) s+=part[i][threadIdx.x]; out[b*512+c]=f2bf(h[b*512+c]+rb(s)); }
}
extern "C" void kernel_launch(void* const* d_in, const int* in_sizes, int n_in, void* d_out, int out_size, void* d_ws, size_t ws_size, hipStream_t stream){
  const u16* x=(const u16*)d_in[0]; const u16* kc=(const u16*)d_in[1]; const u16* vc=(const u16*)d_in[2];
  const u16* wq=(const u16*)d_in[3]; const u16* wo=(const u16*)d_in[4];
  const u16* wg=(const u16*)d_in[6]; const u16* wu=(const u16*)d_in[7]; const u16* wd=(const u16*)d_in[8];
  float* ws=(float*)d_ws; float* q=ws; float* o=ws+16384; float* h=ws+32768; float* act=ws+36864;
  k_q<<<dim3(32,4),256,0,stream>>>(x,wq,q);
  k_attn<<<128,256,0,stream>>>(q,kc,vc,o);
  k_wo<<<dim3(16,4),256,0,stream>>>(o,wo,x,h);
  k_ffn1<<<dim3(43,4),256,0,stream>>>(h,wg,wu,act);
  k_ffn2<<<dim3(16,4),256,0,stream>>>(act,wd,h,(u16*)d_out);
}
```

```cpp
#include <hip/hip_runtime.h>
#include <hip/hip_bf16.h>
#include <stdint.h>

namespace fk {
typedef unsigned short u16;
typedef unsigned u32;
typedef unsigned long long u64;
typedef u32 u32x2 __attribute__((ext_vector_type(2)));
typedef u32 u32x4 __attribute__((ext_vector_type(4)));
typedef float f32x2 __attribute__((ext_vector_type(2)));
typedef float f32x4 __attribute__((ext_vector_type(4)));
typedef short s16x8 __attribute__((ext_vector_type(8)));
typedef __bf16 bf16x2 __attribute__((ext_vector_type(2)));
#define GAS __attribute__((address_space(1)))
#define RLX_AGENT __ATOMIC_RELAXED, __HIP_MEMORY_SCOPE_AGENT

constexpr int NWG = 256, NTHR = 512, JF = 43;
constexpr int SK = 8192, WIN = 2048, FF = 1376;
constexpr u32 WS_EPOCH = 0, WS_ML = 4096, WS_HG = 8192, WS_Y = 65536, WS_PO = WS_Y + (1u << 20), WS_STAMP = WS_PO + (1u << 20),
              WS_BYTES = WS_STAMP + (1u << 20);
constexpr int AUX_SC1 = 16, AUX_NT = 2;
constexpr u32 SPIN_CAP = 1u << 18;
constexpr int L_XN = 0;
constexpr int L_QP = 8192;
constexpr int L_OB = L_QP + 4096;
constexpr int L_MW = L_OB + 4352;
constexpr int L_TAG = L_MW + 64;
constexpr int L_XB = L_TAG + 16;
constexpr int L_PT = L_XB + 4160;
constexpr int L_AC = L_PT + 8192;
constexpr int L_BYTES = L_AC + 320;

template <int I, int N, class F> __device__ __forceinline__ void sfor(F&& f) {
  if constexpr (I < N) { f(std::integral_constant<int, I>{}); sfor<I + 1, N>(f); }
}
__device__ __forceinline__ float asf(u32 x) { return __builtin_bit_cast(float, x); }
__device__ __forceinline__ u32 asu(float x) { return __builtin_bit_cast(u32, x); }
__device__ __forceinline__ float lo2f(u32 x) { return asf(x << 16); }
__device__ __forceinline__ float hi2f(u32 x) { return asf(x & 0xffff0000u); }
__device__ __forceinline__ u32 cvtpk(float lo, float hi) {
  u32 r; asm("v_cvt_pk_bf16_f32 %0, %1, %2" : "=v"(r) : "v"(lo), "v"(hi)); return r;
}
__device__ __forceinline__ float rb(float x) { return lo2f(cvtpk(x, x)); }
__device__ __forceinline__ float dot2(u32 k, u32 q, float acc) {
  return __builtin_amdgcn_fdot2_f32_bf16(__builtin_bit_cast(bf16x2, k), __builtin_bit_cast(bf16x2, q), acc, false);
}
template <int CTRL> __device__ __forceinline__ float dpp(float x) {
  return asf((u32)__builtin_amdgcn_mov_dpp((int)asu(x), CTRL, 0xf, 0xf, true));
}
constexpr int XOR1 = 0xB1, XOR2 = 0x4E, XOR7 = 0x141, XOR8 = 0x128;
__device__ __forceinline__ float xrow_sum(float x) {
  auto s = __builtin_amdgcn_permlane16_swap(asu(x), asu(x), false, false);
  x = asf(s[0]) + asf(s[1]);
  auto t = __builtin_amdgcn_permlane32_swap(asu(x), asu(x), false, false);
  return asf(t[0]) + asf(t[1]);
}
__device__ __forceinline__ float xrow_max(float x) {
  auto s = __builtin_amdgcn_permlane16_swap(asu(x), asu(x), false, false);
  x = fmaxf(asf(s[0]), asf(s[1]));
  auto t = __builtin_amdgcn_permlane32_swap(asu(x), asu(x), false, false);
  return fmaxf(asf(t[0]), asf(t[1]));
}
__device__ __forceinline__ float wave_sum(float x) {
  x += dpp<XOR1>(x); x += dpp<XOR2>(x); x += dpp<XOR7>(x); x += dpp<XOR8>(x);
  return xrow_sum(x);
}
__device__ __forceinline__ float wave_max(float x) {
  x = fmaxf(x, dpp<XOR1>(x)); x = fmaxf(x, dpp<XOR2>(x)); x = fmaxf(x, dpp<XOR7>(x)); x = fmaxf(x, dpp<XOR8>(x));
  return xrow_max(x);
}
__device__ __forceinline__ f32x4 mfma16(u32x4 a, u32x4 b, f32x4 c) {
  return __builtin_amdgcn_mfma_f32_16x16x32_bf16(__builtin_bit_cast(s16x8, a), __builtin_bit_cast(s16x8, b), c, 0, 0, 0);
}
__device__ __forceinline__ u32 plo(u32 r0, u32 r1) { return __builtin_amdgcn_perm(r1, r0, 0x05040100u); }
__device__ __forceinline__ u32 phi(u32 r0, u32 r1) { return __builtin_amdgcn_perm(r1, r0, 0x07060302u); }
__device__ __forceinline__ u32x4 ldnt(const u16* p) { return __builtin_nontemporal_load((const GAS u32x4*)p); }
__device__ __forceinline__ u32x4 ld16(const u16* p) { return *(const GAS u32x4*)p; }
__device__ __forceinline__ u32x2 ld8(const u16* p) { return *(const GAS u32x2*)p; }
__device__ __forceinline__ u32 ld4(const u16* p) { return *(const GAS u32*)p; }
#define SB() __builtin_amdgcn_sched_barrier(0)
__device__ __forceinline__ __amdgpu_buffer_rsrc_t mkrs(const void* p, int bytes) {
  return __builtin_amdgcn_make_buffer_rsrc((void*)p, 0, bytes, 0x00020000);
}

__device__ __forceinline__ u32x4 ln512(const float (&v)[8]) {
  float s = ((v[0] + v[1]) + (v[2] + v[3])) + ((v[4] + v[5]) + (v[6] + v[7]));
  const float mean = wave_sum(s) * (1.f / 512.f);
  float q = 0.f;
#pragma unroll
  for (int i = 0; i < 8; ++i) { const float d = v[i] - mean; q = fmaf(d, d, q); }
  const float var = wave_sum(q) * (1.f / 512.f);
  const float m = rb(mean), sd = rb(sqrtf(rb(rb(var) + 1e-5f)));
  float o[8];
#pragma unroll
  for (int i = 0; i < 8; ++i) o[i] = rb(v[i] - m) / sd;
  return u32x4{cvtpk(o[0], o[1]), cvtpk(o[2], o[3]), cvtpk(o[4], o[5]), cvtpk(o[6], o[7])};
}
__device__ __forceinline__ float reduce8(float (&x)[8], int lane) {
#pragma unroll
  for (int h = 0; h < 4; ++h) { const float t1 = x[h] + dpp<XOR7>(x[h]), t2 = x[h + 4] + dpp<XOR7>(x[h + 4]); x[h] = (lane & 4) ? t2 : t1; }
#pragma unroll
  for (int h = 0; h < 2; ++h) { const float t1 = x[h] + dpp<XOR2>(x[h]), t2 = x[h + 2] + dpp<XOR2>(x[h + 2]); x[h] = (lane & 2) ? t2 : t1; }
  { const float t1 = x[0] + dpp<XOR1>(x[0]), t2 = x[1] + dpp<XOR1>(x[1]); x[0] = (lane & 1) ? t2 : t1; }
  float r = x[0]; r += dpp<XOR8>(r);
  return xrow_sum(r);
}

#define STAMP(i) do {} while (0)

__global__ void __launch_bounds__(NTHR, 2)
fused_block(const u16* __restrict__ x, const u16* __restrict__ kc, const u16* __restrict__ vc, const u16* __restrict__ wq,
            const u16* __restrict__ wo, const u16* __restrict__ wg, const u16* __restrict__ wu, const u16* __restrict__ wd,
            u16* __restrict__ out, unsigned char* __restrict__ ws) {
  __shared__ __attribute__((aligned(16))) unsigned char lds[L_BYTES];
  const int tid = threadIdx.x, lane = tid & 63, w = __builtin_amdgcn_readfirstlane(tid >> 6), g = lane >> 4, c = lane & 15;
  const int bid = blockIdx.x;
  const int h = (bid & 7) + 8 * ((bid >> 3) & 3), bs = bid >> 5, b = bs >> 1, s = bs & 1, pp = 2 * h + s;
  const int rb_ = bid >> 6, rcs = bid & 63;
  const bool is_ffn = bid < JF;
  const __amdgpu_buffer_rsrc_t rs = __builtin_amdgcn_make_buffer_rsrc((void*)ws, 0, (int)WS_BYTES, 0x00020000);
  STAMP(0);
  u32 epoch = 0;
  if (w == 0) epoch = __hip_atomic_load((GAS u32*)(ws + WS_EPOCH), RLX_AGENT);
  const u32x4 xr = ld16(x + b * 512 + 8 * lane);
  SB();
  u32x4 A[16], R[16];
  {
    const __amdgpu_buffer_rsrc_t rq = mkrs(wq + (size_t)(64 * w) * 4096 + h * 128, 64 * 8192);
    const u32 vo = (u32)(16 * g * 8192 + 16 * c);
#pragma unroll
    for (int t = 0; t < 16; ++t) { A[t] = __builtin_amdgcn_raw_buffer_load_b128(rq, vo, t * 8192, 0); SB(); }
  }
  const size_t kvoff = ((size_t)(b * 32 + h) * SK + (SK - WIN) + s * 1024 + w * 128) * 128;
  const __amdgpu_buffer_rsrc_t rk = mkrs(kc + kvoff, 32768), rv = mkrs(vc + kvoff, 32768);
  const u32 lo16 = (u32)lane * 16u;
#define LDK(t) __builtin_amdgcn_raw_buffer_load_b128(rk, lo16 + ((t) & 3) * 1024, ((t) >> 2) * 4096, AUX_NT)
#define LDV(t) __builtin_amdgcn_raw_buffer_load_b128(rv, lo16 + ((t) & 3) * 1024, ((t) >> 2) * 4096, AUX_NT)
#pragma unroll
  for (int t = 0; t < 16; ++t) { R[t] = LDK(t); SB(); }
  if (tid == 0) *(u32*)(lds + L_TAG) = epoch;
  {
    float v[8] = {lo2f(xr[0]), hi2f(xr[0]), lo2f(xr[1]), hi2f(xr[1]), lo2f(xr[2]), hi2f(xr[2]), lo2f(xr[3]), hi2f(xr[3])};
    const u32x4 xn = ln512(v);
    *(u32x4*)(lds + L_XN + w * 1024 + lane * 16) = xn;
  }
  u32x4 xa, xb;
  {
    const unsigned char* p = lds + L_XN + w * 1024 + (64 * w + 16 * g) * 2;
    __builtin_amdgcn_fence(__ATOMIC_RELEASE, "wavefront");
    xa = *(const u32x4*)p; xb = *(const u32x4*)(p + 16);
  }
  {
    f32x2 qa[4] = {{0.f, 0.f}, {0.f, 0.f}, {0.f, 0.f}, {0.f, 0.f}};
#pragma unroll
    for (int t = 0; t < 16; ++t) {
      const u32 pk = t < 8 ? xa[(t >> 1) & 3] : xb[(t >> 1) & 3];
      const float xv = (t & 1) ? hi2f(pk) : lo2f(pk);
#pragma unroll
      for (int k = 0; k < 4; ++k) qa[k] = __builtin_elementwise_fma(f32x2{lo2f(A[t][k]), hi2f(A[t][k])}, f32x2{xv, xv}, qa[k]);
    }
    f32x4 r0 = {xrow_sum(qa[0].x), xrow_sum(qa[0].y), xrow_sum(qa[1].x), xrow_sum(qa[1].y)};
    f32x4 r1 = {xrow_sum(qa[2].x), xrow_sum(qa[2].y), xrow_sum(qa[3].x), xrow_sum(qa[3].y)};
    if (g == 0) { f32x4* qp = (f32x4*)(lds + L_QP + w * 512 + c * 32); qp[0] = r0; qp[1] = r1; }
  }
  STAMP(1);
  asm volatile("" ::: "memory");
  SB();
  u32x2 WO[32];
  {
    const __amdgpu_buffer_rsrc_t ro = mkrs(wo + (size_t)(h * 128) * 512 + 64 * w, 128 * 1024);
    const u32 vo = (u32)(8 * g * 1024 + 8 * c);
#pragma unroll
    for (int i = 0; i < 32; ++i) { WO[i] = __builtin_amdgcn_raw_buffer_load_b64(ro, vo + (i & 3) * 1024, (i >> 3) * 32768 + ((i >> 2) & 1) * 4096, 0); SB(); }
  }
  __syncthreads();
  const u32 tag = *(const u32*)(lds + L_TAG) + 1u;
  u32x4 qf;
  {
    const f32x4* p0 = (const f32x4*)(lds + L_QP + (2 * g) * 512 + c * 32);
    const f32x4* p1 = (const f32x4*)(lds + L_QP + (2 * g + 1) * 512 + c * 32);
    const f32x4 a0 = p0[0] + p1[0], a1 = p0[1] + p1[1];
    qf = u32x4{cvtpk(xrow_sum(a0.x), xrow_sum(a0.y)), cvtpk(xrow_sum(a0.z), xrow_sum(a0.w)),
               cvtpk(xrow_sum(a1.x), xrow_sum(a1.y)), cvtpk(xrow_sum(a1.z), xrow_sum(a1.w))};
  }
  STAMP(2);
  float sc[2];
#pragma unroll
  for (int G = 0; G < 2; ++G) {
    float xs[16];
#pragma unroll
    for (int t = 0; t < 16; ++t) {
      float d = 0.f;
#pragma unroll
      for (int k = 0; k < 4; ++k) d = dot2(R[t][k], qf[k], d);
      asm volatile("" : "+v"(d)::"memory");
      xs[t] = d;
      SB();
      R[t] = G == 0 ? LDK(16 + t) : LDV(t);
      SB();
    }
#define TR_STEP(HALF, CTRL, BIT) _Pragma("unroll") for (int hh = 0; hh < (HALF); ++hh) { \
      const float t1_ = xs[hh] + dpp<CTRL>(xs[hh]), t2_ = xs[hh + (HALF)] + dpp<CTRL>(xs[hh + (HALF)]); xs[hh] = (c & (BIT)) ? t2_ : t1_; }
    TR_STEP(8, XOR8, 8) TR_STEP(4, XOR7, 4) TR_STEP(2, XOR2, 2) TR_STEP(1, XOR1, 1)
#undef TR_STEP
    sc[G] = rb(rb(xs[0]) * 0.08838834764831845f);
    asm volatile("" : "+v"(sc[G])::"memory");
    SB();
  }
  STAMP(3);
  u32x4 BO[16];
#pragma unroll
  for (int ks = 0; ks < 4; ++ks)
#pragma unroll
    for (int e = 0; e < 4; ++e)
#pragma unroll
      for (int jj = 0; jj < 4; ++jj) {
        const u32 r0 = WO[ks * 8 + 2 * jj][e >> 1], r1 = WO[ks * 8 + 2 * jj + 1][e >> 1];
        BO[ks * 4 + e][jj] = (e & 1) ? phi(r0, r1) : plo(r0, r1);
      }
#pragma unroll
  for (int i = 0; i < 16; ++i) asm volatile("" : "+v"(BO[i]));
  SB();
  u32 WG[16], WU[16];
  u32x2 WD[8];
  {
    const int fj = is_ffn ? bid : 0;
    const __amdgpu_buffer_rsrc_t rg = mkrs(wg + (size_t)(64 * w) * FF + 32 * fj, is_ffn ? 64 * FF * 2 : 0), ru = mkrs(wu + (size_t)(64 * w) * FF + 32 * fj, is_ffn ? 64 * FF * 2 : 0);
    const __amdgpu_buffer_rsrc_t rd = mkrs(wd + (size_t)(32 * fj) * 512 + 64 * w, is_ffn ? 32 * 1024 : 0);
    const u32 vo = (u32)(8 * g * FF * 2 + 4 * c), vd = (u32)(8 * g * 1024 + 8 * c);
#pragma unroll
    for (int i = 0; i < 16; ++i) {
      WG[i] = __builtin_amdgcn_raw_buffer_load_b32(rg, vo, ((i >> 3) * 32 + (i & 7)) * FF * 2, 0); SB();
      WU[i] = __builtin_amdgcn_raw_buffer_load_b32(ru, vo, ((i >> 3) * 32 + (i & 7)) * FF * 2, 0); SB();
    }
#pragma unroll
    for (int i = 0; i < 8; ++i) { WD[i] = __builtin_amdgcn_raw_buffer_load_b64(rd, vd + (i & 3) * 1024, (i >> 2) * 4096, 0); SB(); }
  }
  const float mw = wave_max(fmaxf(sc[0], sc[1]));
  float p[2];
  p[0] = __builtin_amdgcn_exp2f((sc[0] - mw) * 1.4426950408889634f);
  p[1] = __builtin_amdgcn_exp2f((sc[1] - mw) * 1.4426950408889634f);
  const float lw = wave_sum(p[0] + p[1]);
  f32x2 oa[4] = {{0.f, 0.f}, {0.f, 0.f}, {0.f, 0.f}, {0.f, 0.f}};
  sfor<0, 16>([&](auto I) {
    constexpr int t = I;
    const float pb = asf((u32)__builtin_amdgcn_update_dpp(0, (int)asu(p[0]), 0x150 + t, 0xf, 0xf, true));
    const u32x4 v = R[t];
#pragma unroll
    for (int k = 0; k < 4; ++k) oa[k] = __builtin_elementwise_fma(f32x2{lo2f(v[k]), hi2f(v[k])}, f32x2{pb, pb}, oa[k]);
    asm volatile("" : "+v"(oa[0]), "+v"(oa[1]), "+v"(oa[2]), "+v"(oa[3])::"memory");
    SB();
    R[t] = LDV(16 + t);
    SB();
  });
  STAMP(4);
  sfor<0, 16>([&](auto I) {
    constexpr int t = I;
    const float pb = asf((u32)__builtin_amdgcn_update_dpp(0, (int)asu(p[1]), 0x150 + t, 0xf, 0xf, true));
    const u32x4 v = R[t];
#pragma unroll
    for (int k = 0; k < 4; ++k) oa[k] = __builtin_elementwise_fma(f32x2{lo2f(v[k]), hi2f(v[k])}, f32x2{pb, pb}, oa[k]);
    asm volatile("" : "+v"(oa[0]), "+v"(oa[1]), "+v"(oa[2]), "+v"(oa[3]));
    SB();
  });
  STAMP(5);
  {
    float o[8] = {xrow_sum(oa[0].x), xrow_sum(oa[0].y), xrow_sum(oa[1].x), xrow_sum(oa[1].y),
                  xrow_sum(oa[2].x), xrow_sum(oa[2].y), xrow_sum(oa[3].x), xrow_sum(oa[3].y)};
    u32x4 hi = {cvtpk(o[0], o[1]), cvtpk(o[2], o[3]), cvtpk(o[4], o[5]), cvtpk(o[6], o[7])};
    u32x4 lo = {cvtpk(o[0] - lo2f(hi[0]), o[1] - hi2f(hi[0])), cvtpk(o[2] - lo2f(hi[1]), o[3] - hi2f(hi[1])),
                cvtpk(o[4] - lo2f(hi[2]), o[5] - hi2f(hi[2])), cvtpk(o[6] - lo2f(hi[3]), o[7] - hi2f(hi[3]))};
    if (g < 2) *(u32x4*)(lds + L_OB + (2 * w + g) * 272 + c * 16) = g ? lo : hi;
    if (lane == 0) { *(float*)(lds + L_MW + w * 4) = mw; *(float*)(lds + L_MW + 32 + w * 4) = lw; }
  }
  __syncthreads();
  {
    u32x4 af[4];
#pragma unroll
    for (int ks = 0; ks < 4; ++ks) af[ks] = *(const u32x4*)(lds + L_OB + c * 272 + (32 * ks + 8 * g) * 2);
    const f32x4 m0 = *(const f32x4*)(lds + L_MW), m1 = *(const f32x4*)(lds + L_MW + 16);
    const f32x4 l0 = *(const f32x4*)(lds + L_MW + 32), l1 = *(const f32x4*)(lds + L_MW + 48);
    const float M = fmaxf(fmaxf(fmaxf(m0.x, m0.y), fmaxf(m0.z, m0.w)), fmaxf(fmaxf(m1.x, m1.y), fmaxf(m1.z, m1.w)));
    const f32x2 mg = *(const f32x2*)(lds + L_MW + 8 * g);
    const float LOG2E = 1.4426950408889634f;
    const float s0 = __builtin_amdgcn_exp2f((mg.x - M) * LOG2E), s1 = __builtin_amdgcn_exp2f((mg.y - M) * LOG2E);
    f32x4 d[4];
#pragma unroll
    for (int e = 0; e < 4; ++e) d[e] = f32x4{0.f, 0.f, 0.f, 0.f};
#pragma unroll
    for (int ks = 0; ks < 4; ++ks)
#pragma unroll
      for (int e = 0; e < 4; ++e) d[e] = mfma16(af[ks], BO[ks * 4 + e], d[e]);
    float y[4];
#pragma unroll
    for (int e = 0; e < 4; ++e) y[e] = xrow_sum(fmaf(s0, d[e].x + d[e].y, s1 * (d[e].z + d[e].w)));
    const u32 off = WS_Y + (u32)(((b * 64 + 8 * w + (c >> 1)) * 64 + pp) * 64 + ((c & 1) * 4 + 2 * g) * 8);
    const u32x4 gv = {asu(g ? y[2] : y[0]), tag, asu(g ? y[3] : y[1]), tag};
    if (g < 2) __builtin_amdgcn_raw_buffer_store_b128(gv, rs, off, 0, AUX_SC1);
    if (tid == 0) {
      const float L = l0.x * __builtin_amdgcn_exp2f((m0.x - M) * LOG2E) + l0.y * __builtin_amdgcn_exp2f((m0.y - M) * LOG2E) +
                      l0.z * __builtin_amdgcn_exp2f((m0.z - M) * LOG2E) + l0.w * __builtin_amdgcn_exp2f((m0.w - M) * LOG2E) +
                      l1.x * __builtin_amdgcn_exp2f((m1.x - M) * LOG2E) + l1.y * __builtin_amdgcn_exp2f((m1.y - M) * LOG2E) +
                      l1.z * __builtin_amdgcn_exp2f((m1.z - M) * LOG2E) + l1.w * __builtin_amdgcn_exp2f((m1.w - M) * LOG2E);
      const u32x4 ml = {asu(M), tag, asu(L), tag};
      __builtin_amdgcn_raw_buffer_store_b128(ml, rs, WS_ML + (u32)((b * 64 + pp) * 16), 0, AUX_SC1);
    }
  }
  STAMP(6);
  if (w != 7 && !is_ffn) return;
  float hval = 0.f;
  if (w == 7) {
    const u32 yo = WS_Y + (u32)(((rb_ * 64 + rcs) * 64 + lane) * 64), mo = WS_ML + (u32)((rb_ * 64 + lane) * 16);
    u32x4 a0, a1, a2, a3, ml;
    const u32 xres = x[rb_ * 512 + 8 * rcs + (lane & 7)];
    for (u32 spin = 0;; ++spin) {
      asm volatile("" ::: "memory");
      a0 = __builtin_amdgcn_raw_buffer_load_b128(rs, yo, 0, AUX_SC1);
      a1 = __builtin_amdgcn_raw_buffer_load_b128(rs, yo + 16, 0, AUX_SC1);
      a2 = __builtin_amdgcn_raw_buffer_load_b128(rs, yo + 32, 0, AUX_SC1);
      a3 = __builtin_amdgcn_raw_buffer_load_b128(rs, yo + 48, 0, AUX_SC1);
      ml = __builtin_amdgcn_raw_buffer_load_b128(rs, mo, 0, AUX_SC1);
      const bool ok = a0.y == tag && a0.w == tag && a1.y == tag && a1.w == tag && a2.y == tag && a2.w == tag && a3.y == tag &&
                      a3.w == tag && ml.y == tag && ml.w == tag;
      if (__all(ok) || spin > SPIN_CAP) break;
      __builtin_amdgcn_s_sleep(1);
    }
    STAMP(7);
    const float LOG2E = 1.4426950408889634f;
    const float Ms = asf(ml.x), Ls = asf(ml.z), Mo = dpp<XOR1>(Ms), Lo = dpp<XOR1>(Ls);
    const float M = fmaxf(Ms, Mo), es = __builtin_amdgcn_exp2f((Ms - M) * LOG2E), eo = __builtin_amdgcn_exp2f((Mo - M) * LOG2E);
    const float wgt = es / fmaf(Ls, es, Lo * eo);
    float v[8] = {asf(a0.x) * wgt, asf(a0.z) * wgt, asf(a1.x) * wgt, asf(a1.z) * wgt, asf(a2.x) * wgt, asf(a2.z) * wgt, asf(a3.x) * wgt, asf(a3.z) * wgt};
    const float attn = reduce8(v, lane);
    hval = rb(lo2f(xres) + rb(attn));
    const float hn = dpp<XOR1>(hval);
    const u32x2 hg = {cvtpk(hval, hn), tag};
    if (lane < 8 && !(lane & 1)) __builtin_amdgcn_raw_buffer_store_b64(hg, rs, WS_HG + (u32)((rb_ * 256 + 4 * rcs + (lane >> 1)) * 8), 0, AUX_SC1);
    STAMP(8);
  }
  if (is_ffn) {
    u32x4 BG[4], BU[4], BD[4];
#pragma unroll
    for (int ks = 0; ks < 2; ++ks)
#pragma unroll
      for (int e = 0; e < 2; ++e)
#pragma unroll
        for (int jj = 0; jj < 4; ++jj) {
          const u32 g0 = WG[ks * 8 + 2 * jj], g1 = WG[ks * 8 + 2 * jj + 1], u0 = WU[ks * 8 + 2 * jj], u1 = WU[ks * 8 + 2 * jj + 1];
          BG[ks * 2 + e][jj] = e ? phi(g0, g1) : plo(g0, g1);
          BU[ks * 2 + e][jj] = e ? phi(u0, u1) : plo(u0, u1);
        }
#pragma unroll
    for (int e = 0; e < 4; ++e)
#pragma unroll
      for (int jj = 0; jj < 4; ++jj) {
        const u32 r0 = WD[2 * jj][e >> 1], r1 = WD[2 * jj + 1][e >> 1];
        BD[e][jj] = (e & 1) ? phi(r0, r1) : plo(r0, r1);
      }
#pragma unroll
    for (int i = 0; i < 4; ++i) asm volatile("" : "+v"(BG[i]), "+v"(BU[i]), "+v"(BD[i]));
    SB();
    if (w < 4) {
      const u32 ho = WS_HG + (u32)((w * 256 + 4 * lane) * 8);
      u32x4 a0, a1;
      for (u32 spin = 0;; ++spin) {
        asm volatile("" ::: "memory");
        a0 = __builtin_amdgcn_raw_buffer_load_b128(rs, ho, 0, AUX_SC1);
        a1 = __builtin_amdgcn_raw_buffer_load_b128(rs, ho + 16, 0, AUX_SC1);
        const bool ok = a0.y == tag && a0.w == tag && a1.y == tag && a1.w == tag;
        if (__all(ok) || spin > SPIN_CAP) break;
        __builtin_amdgcn_s_sleep(1);
      }
      STAMP(7);
      float v[8] = {lo2f(a0.x), hi2f(a0.x), lo2f(a0.z), hi2f(a0.z), lo2f(a1.x), hi2f(a1.x), lo2f(a1.z), hi2f(a1.z)};
      *(u32x4*)(lds + L_XB + w * 1040 + lane * 16) = ln512(v);
    }
    __syncthreads();
    {
      f32x4 dg[2] = {{0.f, 0.f, 0.f, 0.f}, {0.f, 0.f, 0.f, 0.f}}, du[2] = {{0.f, 0.f, 0.f, 0.f}, {0.f, 0.f, 0.f, 0.f}};
#pragma unroll
      for (int ks = 0; ks < 2; ++ks) {
        const u32x4 af = *(const u32x4*)(lds + L_XB + (c & 3) * 1040 + (64 * w + 32 * ks + 8 * g) * 2);
#pragma unroll
        for (int e = 0; e < 2; ++e) { dg[e] = mfma16(af, BG[ks * 2 + e], dg[e]); du[e] = mfma16(af, BU[ks * 2 + e], du[e]); }
      }
      if (g == 0) {
        f32x4* pt = (f32x4*)(lds + L_PT + w * 1024 + c * 16);
        pt[0] = dg[0]; pt[16] = dg[1]; pt[32] = du[0]; pt[48] = du[1];
      }
    }
    __syncthreads();
    if (tid < 128) {
      const int i = tid & 15, bb = (tid >> 4) & 3, eo = tid >> 6;
      const float* pt = (const float*)(lds + L_PT + eo * 256 + i * 16 + bb * 4);
      float gs = 0.f, us = 0.f;
#pragma unroll
      for (int ww = 0; ww < 8; ++ww) { gs += pt[ww * 256]; us += pt[ww * 256 + 128]; }
      gs = rb(gs); us = rb(us);
      const float si = rb(gs / (1.f + __expf(-gs)));
      *(u16*)(lds + L_AC + bb * 80 + (2 * i + eo) * 2) = (u16)(cvtpk(si * us, 0.f) & 0xffffu);
    }
    __syncthreads();
    {
      const u32x4 af = *(const u32x4*)(lds + L_AC + (c & 3) * 80 + g * 16);
      f32x4 d[4];
#pragma unroll
      for (int e = 0; e < 4; ++e) d[e] = mfma16(af, BD[e], f32x4{0.f, 0.f, 0.f, 0.f});
      if (g == 0) {
#pragma unroll
        for (int bb = 0; bb < 4; ++bb) {
          const u32 off = WS_PO + (u32)(((bb * 64 + 8 * w + (c >> 1)) * 64 + bid) * 64 + (c & 1) * 32);
          __builtin_amdgcn_raw_buffer_store_b128(u32x4{asu(d[0][bb]), tag, asu(d[1][bb]), tag}, rs, off, 0, AUX_SC1);
          __builtin_amdgcn_raw_buffer_store_b128(u32x4{asu(d[2][bb]), tag, asu(d[3][bb]), tag}, rs, off + 16, 0, AUX_SC1);
        }
      }
    }
    STAMP(9);
  }
  if (w == 7) {
    const int jl = lane < JF ? lane : 0;
    const u32 po = WS_PO + (u32)(((rb_ * 64 + rcs) * 64 + jl) * 64);
    u32x4 a0, a1, a2, a3;
    for (u32 spin = 0;; ++spin) {
      asm volatile("" ::: "memory");
      a0 = __builtin_amdgcn_raw_buffer_load_b128(rs, po, 0, AUX_SC1);
      a1 = __builtin_amdgcn_raw_buffer_load_b128(rs, po + 16, 0, AUX_SC1);
      a2 = __builtin_amdgcn_raw_buffer_load_b128(rs, po + 32, 0, AUX_SC1);
      a3 = __builtin_amdgcn_raw_buffer_load_b128(rs, po + 48, 0, AUX_SC1);
      const bool ok = a0.y == tag && a0.w == tag && a1.y == tag && a1.w == tag && a2.y == tag && a2.w == tag && a3.y == tag && a3.w == tag;
      if (__all(ok) || spin > SPIN_CAP) break;
      __builtin_amdgcn_s_sleep(1);
    }
    STAMP(10);
    const float z = lane < JF ? 1.f : 0.f;
    float v[8] = {asf(a0.x) * z, asf(a0.z) * z, asf(a1.x) * z, asf(a1.z) * z, asf(a2.x) * z, asf(a2.z) * z, asf(a3.x) * z, asf(a3.z) * z};
    const float f = reduce8(v, lane);
    const float o = hval + rb(f);
    const float on = dpp<XOR1>(o);
    if (lane < 8 && !(lane & 1)) *(GAS u32*)(out + rb_ * 512 + 8 * rcs + lane) = cvtpk(o, on);
    if (bid == 0 && lane == 0) __hip_atomic_store((GAS u32*)(ws + WS_EPOCH), tag, RLX_AGENT);
    STAMP(11);
  }
}
}

extern "C" void kernel_launch(void* const* d_in, const int* in_sizes, int n_in, void* d_out, int out_size, void* d_ws, size_t ws_size,
                              hipStream_t stream) {
  using namespace fk;
  fused_block<<<NWG, NTHR, 0, stream>>>((const u16*)d_in[0], (const u16*)d_in[1], (const u16*)d_in[2], (const u16*)d_in[3],
                                        (const u16*)d_in[4], (const u16*)d_in[6], (const u16*)d_in[7], (const u16*)d_in[8],
                                        (u16*)d_out, (unsigned char*)d_ws);
}
```

```cpp
#include <hip/hip_runtime.h>
#include <hip/hip_bf16.h>
#include <stdint.h>

namespace fk {
typedef unsigned short u16;
typedef unsigned u32;
typedef unsigned long long u64;
typedef u32 u32x2 __attribute__((ext_vector_type(2)));
typedef u32 u32x4 __attribute__((ext_vector_type(4)));
typedef float f32x2 __attribute__((ext_vector_type(2)));
typedef float f32x4 __attribute__((ext_vector_type(4)));
typedef short s16x8 __attribute__((ext_vector_type(8)));
typedef __bf16 bf16x2 __attribute__((ext_vector_type(2)));
#define GAS __attribute__((address_space(1)))
#define RLX_AGENT __ATOMIC_RELAXED, __HIP_MEMORY_SCOPE_AGENT

constexpr int NWG = 256, NTHR = 512, JF = 43;
#define BK1 24
#define BK3 24
#define PREF 1
#define PREF_AT 8
#define FAUX 0
#define RD 24

constexpr int SK = 8192, WIN = 2048, FF = 1376;
constexpr u32 WS_EPOCH = 0, WS_ST = 32768, WS_HG = 65536, WS_Y = 131072, WS_PO = WS_Y + (1u << 20), WS_STAMP = WS_PO + (1u << 20),
              WS_BYTES = WS_STAMP + (1u << 20);
constexpr int AUX_SC1 = 16, AUX_NT = 2;
constexpr u32 SPIN_CAP = 1u << 18;
constexpr int L_XN = 0;
constexpr int L_QP = 8192;
constexpr int L_OB = L_QP + 4096;
constexpr int L_MW = L_OB + 4352;
constexpr int L_TAG = L_MW + 80;
constexpr int L_XB = L_TAG + 16;
constexpr int L_PT = L_XB + 4160;
constexpr int L_AC = L_PT + 8192;
constexpr int L_BYTES = L_AC + 320;

template <int I, int N, class F> __device__ __forceinline__ void sfor(F&& f) {
  if constexpr (I < N) { f(std::integral_constant<int, I>{}); sfor<I + 1, N>(f); }
}
__device__ __forceinline__ float asf(u32 x) { return __builtin_bit_cast(float, x); }
__device__ __forceinline__ u32 asu(float x) { return __builtin_bit_cast(u32, x); }
__device__ __forceinline__ float lo2f(u32 x) { return asf(x << 16); }
__device__ __forceinline__ float hi2f(u32 x) { return asf(x & 0xffff0000u); }
__device__ __forceinline__ u32 cvtpk(float lo, float hi) {
  return __builtin_bit_cast(u32, __builtin_convertvector(f32x2{lo, hi}, bf16x2));
}
__device__ __forceinline__ float rb(float x) { return lo2f(cvtpk(x, x)); }
__device__ __forceinline__ float dot2(u32 k, u32 q, float acc) {
  return __builtin_amdgcn_fdot2_f32_bf16(__builtin_bit_cast(bf16x2, k), __builtin_bit_cast(bf16x2, q), acc, false);
}
template <int CTRL> __device__ __forceinline__ float dpp(float x) {
  return asf((u32)__builtin_amdgcn_mov_dpp((int)asu(x), CTRL, 0xf, 0xf, true));
}
constexpr int XOR1 = 0xB1, XOR2 = 0x4E, XOR7 = 0x141, XOR8 = 0x128;
__device__ __forceinline__ float xrow_sum(float x) {
  auto s = __builtin_amdgcn_permlane16_swap(asu(x), asu(x), false, false);
  x = asf(s[0]) + asf(s[1]);
  auto t = __builtin_amdgcn_permlane32_swap(asu(x), asu(x), false, false);
  return asf(t[0]) + asf(t[1]);
}
__device__ __forceinline__ float xrow_max(float x) {
  auto s = __builtin_amdgcn_permlane16_swap(asu(x), asu(x), false, false);
  x = fmaxf(asf(s[0]), asf(s[1]));
  auto t = __builtin_amdgcn_permlane32_swap(asu(x), asu(x), false, false);
  return fmaxf(asf(t[0]), asf(t[1]));
}
__device__ __forceinline__ float wave_sum(float x) {
  x += dpp<XOR1>(x); x += dpp<XOR2>(x); x += dpp<XOR7>(x); x += dpp<XOR8>(x);
  return xrow_sum(x);
}
__device__ __forceinline__ float wave_max(float x) {
  x = fmaxf(x, dpp<XOR1>(x)); x = fmaxf(x, dpp<XOR2>(x)); x = fmaxf(x, dpp<XOR7>(x)); x = fmaxf(x, dpp<XOR8>(x));
  return xrow_max(x);
}
__device__ __forceinline__ f32x4 mfma16(u32x4 a, u32x4 b, f32x4 c) {
  return __builtin_amdgcn_mfma_f32_16x16x32_bf16(__builtin_bit_cast(s16x8, a), __builtin_bit_cast(s16x8, b), c, 0, 0, 0);
}
__device__ __forceinline__ u32 plo(u32 r0, u32 r1) { return __builtin_amdgcn_perm(r1, r0, 0x05040100u); }
__device__ __forceinline__ u32 phi(u32 r0, u32 r1) { return __builtin_amdgcn_perm(r1, r0, 0x07060302u); }
__device__ __forceinline__ u32x4 ldnt(const u16* p) { return __builtin_nontemporal_load((const GAS u32x4*)p); }
__device__ __forceinline__ u32x4 ld16(const u16* p) { return *(const GAS u32x4*)p; }
__device__ __forceinline__ u32x2 ld8(const u16* p) { return *(const GAS u32x2*)p; }
__device__ __forceinline__ u32 ld4(const u16* p) { return *(const GAS u32*)p; }
#define SB() __builtin_amdgcn_sched_barrier(0)
__device__ __forceinline__ __amdgpu_buffer_rsrc_t mkrs(const void* p, int bytes) {
  return __builtin_amdgcn_make_buffer_rsrc((void*)p, 0, bytes, 0x00020000);
}

__device__ __forceinline__ float sw32(float a, float b) {
  auto s = __builtin_amdgcn_permlane32_swap(asu(a), asu(b), false, false);
  return asf(s[0]) + asf(s[1]);
}
__device__ __forceinline__ float sw16(float a, float b) {
  auto s = __builtin_amdgcn_permlane16_swap(asu(a), asu(b), false, false);
  return asf(s[0]) + asf(s[1]);
}
__device__ __forceinline__ u32x4 ln512(const float (&v)[8]) {
  const float piv = asf((u32)__builtin_amdgcn_readfirstlane((int)asu(v[0])));
  float s = 0.f, q = 0.f;
#pragma unroll
  for (int i = 0; i < 8; ++i) { const float d = v[i] - piv; s += d; q = fmaf(d, d, q); }
  s += dpp<XOR1>(s); q += dpp<XOR1>(q);
  s += dpp<XOR2>(s); q += dpp<XOR2>(q);
  s += dpp<XOR7>(s); q += dpp<XOR7>(q);
  s += dpp<XOR8>(s); q += dpp<XOR8>(q);
  float t = sw32(s, q);
  t = sw16(t, t);
  const float S = asf((u32)__builtin_amdgcn_readlane((int)asu(t), 0)), Q = asf((u32)__builtin_amdgcn_readlane((int)asu(t), 32));
  const float dm = S * (1.f / 512.f);
  const float mean = piv + dm, var = fmaxf(fmaf(-dm, dm, Q * (1.f / 512.f)), 0.f);
  const float m = rb(mean), sd = rb(__builtin_amdgcn_sqrtf(rb(rb(var) + 1e-5f)));
  const float rs = __builtin_amdgcn_rcpf(sd);
  u32x4 o;
#pragma unroll
  for (int i = 0; i < 4; ++i) { const u32 d = cvtpk(v[2 * i] - m, v[2 * i + 1] - m); o[i] = cvtpk(lo2f(d) * rs, hi2f(d) * rs); }
  return o;
}
__device__ __forceinline__ float reduce8(const float (&x)[8], int c) {
  const float y0 = sw32(x[0], x[4]), y1 = sw32(x[1], x[5]), y2 = sw32(x[2], x[6]), y3 = sw32(x[3], x[7]);
  const float z0 = sw16(y0, y2), z1 = sw16(y1, y3);
  const float t1 = z0 + dpp<XOR8>(z0), t2 = z1 + dpp<XOR8>(z1);
  float r = (c & 8) ? t2 : t1;
  r += dpp<XOR7>(r); r += dpp<XOR2>(r); r += dpp<XOR1>(r);
  return r;
}

__device__ __forceinline__ float reduce2q(float v0, float v1) {
  float t = sw32(v0, v1);
  t = sw16(t, t);
  t += dpp<0x124>(t);
  t += dpp<XOR8>(t);
  return t;
}
#define STAMP(i) do {} while (0)

__global__ void __launch_bounds__(NTHR, 2)
fused_block(const u16* __restrict__ x, const u16* __restrict__ kc, const u16* __restrict__ vc, const u16* __restrict__ wq,
            const u16* __restrict__ wo, const u16* __restrict__ wg, const u16* __restrict__ wu, const u16* __restrict__ wd,
            u16* __restrict__ out, unsigned char* __restrict__ ws) {
  __shared__ __attribute__((aligned(16))) unsigned char lds[L_BYTES];
  const int tid = threadIdx.x, lane = tid & 63, w = __builtin_amdgcn_readfirstlane(tid >> 6), g = lane >> 4, c = lane & 15;
  const int bid = blockIdx.x;
  const int h = (bid & 7) + 8 * ((bid >> 3) & 3), bs = bid >> 5, b = bs >> 1, s = bs & 1, pp = 2 * h + s;
  const bool is_ffn = bid < JF;
  const int xg = bid & 7, fstart = xg < 3 ? 6 * xg : 5 * xg + 3, fcnt = xg < 3 ? 6 : 5;
  const int fj = fstart + (bid >> 3);
  const bool is_red = !is_ffn && (w == 7 || (w == 6 && bid < 2 * JF));
  const int task = bid - JF + (w == 6 ? NWG - JF : 0);
  const int rb_ = task >> 6, rcs = task & 63;
  const __amdgpu_buffer_rsrc_t rs = mkrs(ws, (int)WS_BYTES);
  const float LOG2E = 1.4426950408889634f;
  STAMP(0);
  u32 epoch = 0;
  if (w == 0) epoch = __hip_atomic_load((GAS u32*)(ws + WS_EPOCH), RLX_AGENT);
  const u32x4 xr = ld16(x + b * 512 + 8 * lane);
  SB();
  u32x4 A[16];
  {
    const __amdgpu_buffer_rsrc_t rq = mkrs(wq + (size_t)(64 * w) * 4096 + h * 128, 64 * 8192);
    const u32 vo = (u32)(16 * g * 8192 + 16 * c);
#pragma unroll
    for (int t = 0; t < 16; ++t) { A[t] = __builtin_amdgcn_raw_buffer_load_b128(rq, vo, t * 8192, AUX_NT); SB(); }
  }
  const size_t kvoff = ((size_t)(b * 32 + h) * SK + (SK - WIN) + s * 1024 + w * 4) * 128;
  const __amdgpu_buffer_rsrc_t rk = mkrs(kc + kvoff, 262144 - 7168), rv = mkrs(vc + kvoff, 262144 - 7168);
  const u32 lo16 = (u32)lane * 16u;
#define LDK(t) __builtin_amdgcn_raw_buffer_load_b128(rk, lo16, (t) * 8192, AUX_NT)
#define LDV(t) __builtin_amdgcn_raw_buffer_load_b128(rv, lo16, (t) * 8192, AUX_NT)
#define LDN(n) ((n) < 32 ? LDK(n) : LDV((n) - 32))
  u32x4 R[RD];
#pragma unroll
  for (int t = 0; t < RD; ++t) { R[t] = LDN(t); SB(); }
  if (tid == 0) *(u32*)(lds + L_TAG) = epoch;
  {
    const float v[8] = {lo2f(xr[0]), hi2f(xr[0]), lo2f(xr[1]), hi2f(xr[1]), lo2f(xr[2]), hi2f(xr[2]), lo2f(xr[3]), hi2f(xr[3])};
    *(u32x4*)(lds + L_XN + w * 1024 + lane * 16) = ln512(v);
  }
  u32x4 xa, xb;
  {
    const unsigned char* p = lds + L_XN + w * 1024 + (64 * w + 16 * g) * 2;
    __builtin_amdgcn_fence(__ATOMIC_RELEASE, "wavefront");
    xa = *(const u32x4*)p; xb = *(const u32x4*)(p + 16);
  }
  {
    f32x2 qa[4] = {{0.f, 0.f}, {0.f, 0.f}, {0.f, 0.f}, {0.f, 0.f}};
#pragma unroll
    for (int t = 0; t < 16; ++t) {
      const u32 pk = t < 8 ? xa[(t >> 1) & 3] : xb[(t >> 1) & 3];
      const float xv = (t & 1) ? hi2f(pk) : lo2f(pk);
#pragma unroll
      for (int k = 0; k < 4; ++k) qa[k] = __builtin_elementwise_fma(f32x2{lo2f(A[t][k]), hi2f(A[t][k])}, f32x2{xv, xv}, qa[k]);
    }
    const float y0 = sw32(qa[0].x, qa[2].x), y1 = sw32(qa[0].y, qa[2].y), y2 = sw32(qa[1].x, qa[3].x), y3 = sw32(qa[1].y, qa[3].y);
    *(f32x2*)(lds + L_QP + w * 512 + c * 32 + g * 8) = f32x2{sw16(y0, y2), sw16(y1, y3)};
  }
  STAMP(1);
  asm volatile("" ::: "memory");
  SB();
  u32x2 WO[32];
  const __amdgpu_buffer_rsrc_t ro = mkrs(wo + (size_t)(h * 128) * 512 + 64 * w, 128 * 1024);
  const u32 voo = (u32)(8 * g * 1024 + 8 * c);
#define LDO(i) __builtin_amdgcn_raw_buffer_load_b64(ro, voo + ((i) & 3) * 1024, ((i) >> 3) * 32768 + (((i) >> 2) & 1) * 4096, AUX_NT)
#pragma unroll
  for (int i = 0; i < 8; ++i) { WO[i] = LDO(i); SB(); }
  __syncthreads();
  const u32 tag = *(const u32*)(lds + L_TAG) + 1u;
  u32x4 qf;
  {
    const f32x4* p0 = (const f32x4*)(lds + L_QP + (2 * g) * 512 + c * 32);
    const f32x4* p1 = (const f32x4*)(lds + L_QP + (2 * g + 1) * 512 + c * 32);
    const f32x4 a0 = p0[0] + p1[0], a1 = p0[1] + p1[1];
    qf = u32x4{cvtpk(xrow_sum(a0.x), xrow_sum(a0.y)), cvtpk(xrow_sum(a0.z), xrow_sum(a0.w)),
               cvtpk(xrow_sum(a1.x), xrow_sum(a1.y)), cvtpk(xrow_sum(a1.z), xrow_sum(a1.w))};
  }
  STAMP(2);
  float sc[2];
#pragma unroll
  for (int G = 0; G < 2; ++G) {
    float xs[16];
#pragma unroll
    for (int t = 0; t < 16; ++t) {
      float d = 0.f;
#pragma unroll
      for (int k = 0; k < 4; ++k) d = dot2(R[(16 * G + t) % RD][k], qf[k], d);
      asm volatile("" : "+v"(d)::"memory");
      xs[t] = d;
      SB();
      R[(16 * G + t) % RD] = LDN(16 * G + t + RD);
      SB();
      if (16 * G + t < 24) { WO[8 + 16 * G + t] = LDO(8 + 16 * G + t); SB(); }
    }
#define TR_STEP(HALF, CTRL, BIT) _Pragma("unroll") for (int hh = 0; hh < (HALF); ++hh) { \
      const float t1_ = xs[hh] + dpp<CTRL>(xs[hh]), t2_ = xs[hh + (HALF)] + dpp<CTRL>(xs[hh + (HALF)]); xs[hh] = (c & (BIT)) ? t2_ : t1_; }
    TR_STEP(8, XOR8, 8) TR_STEP(4, XOR7, 4) TR_STEP(2, XOR2, 2) TR_STEP(1, XOR1, 1)
#undef TR_STEP
    sc[G] = rb(rb(xs[0]) * 0.08838834764831845f);
    asm volatile("" : "+v"(sc[G])::"memory");
    SB();
  }
  STAMP(3);
  const float mw = wave_max(fmaxf(sc[0], sc[1]));
  float p[2];
  p[0] = __builtin_amdgcn_exp2f((sc[0] - mw) * LOG2E);
  p[1] = __builtin_amdgcn_exp2f((sc[1] - mw) * LOG2E);
  const float lw = wave_sum(p[0] + p[1]);
  if (lane == 0) {
    __builtin_amdgcn_raw_buffer_store_b128(u32x4{asu(mw), tag, asu(lw), tag}, rs, WS_ST + (u32)(((b * 64 + pp) * 8 + w) * 16), 0, AUX_SC1);
    *(float*)(lds + L_MW + w * 4) = mw; *(float*)(lds + L_MW + 32 + w * 4) = lw;
  }
  SB();
  f32x2 oa[4] = {{0.f, 0.f}, {0.f, 0.f}, {0.f, 0.f}, {0.f, 0.f}};
  u32x4 BO[16];
  u32x4 pst = {0u, 0u, 0u, 0u};
  u32 pf0 = 0, pf1 = 0, pf2 = 0;
  const u32 psto = WS_ST + (u32)(((b * 64 + (pp ^ 1)) * 8 + (lane & 7)) * 16);
  sfor<0, 32>([&](auto I) {
    constexpr int t = I;
    if constexpr (t == 24) {
#pragma unroll
      for (int ks = 0; ks < 4; ++ks)
#pragma unroll
        for (int e = 0; e < 4; ++e)
#pragma unroll
          for (int jj = 0; jj < 4; ++jj) {
            const u32 r0 = WO[ks * 8 + 2 * jj][e >> 1], r1 = WO[ks * 8 + 2 * jj + 1][e >> 1];
            BO[ks * 4 + e][jj] = (e & 1) ? phi(r0, r1) : plo(r0, r1);
          }
#pragma unroll
      for (int i = 0; i < 16; ++i) asm volatile("" : "+v"(BO[i]));
      SB();
      STAMP(4);
    }
    const float pb = asf((u32)__builtin_amdgcn_update_dpp(0, (int)asu(p[t >> 4]), 0x150 + (t & 15), 0xf, 0xf, true));
    const u32x4 v = R[(32 + t) % RD];
#pragma unroll
    for (int k = 0; k < 4; ++k) oa[k] = __builtin_elementwise_fma(f32x2{lo2f(v[k]), hi2f(v[k])}, f32x2{pb, pb}, oa[k]);
    asm volatile("" : "+v"(oa[0]), "+v"(oa[1]), "+v"(oa[2]), "+v"(oa[3])::"memory");
    SB();
    if constexpr (32 + t + RD < 64) { R[(32 + t) % RD] = LDN(32 + t + RD); SB(); }
    if constexpr (t == PREF_AT) {
      const int wi = (bid >> 3) * 8 + w, np = 4 * fcnt;
      const int rr = lane >= np ? 1 : 0, pc = lane - rr * np;
      const bool on = lane < 2 * np;
      const u32 go = (u32)((2 * wi + rr) * FF * 2 + fstart * 64 + pc * 16), dn = (u32)(fstart * 32768 + wi * (fcnt * 128) + lane * 16);
      const __amdgpu_buffer_rsrc_t rg = mkrs(wg, 512 * FF * 2), ru = mkrs(wu, 512 * FF * 2), rd = mkrs(wd, FF * 1024);
      if (on) {
        const u32x4 z0 = __builtin_amdgcn_raw_buffer_load_b128(rg, go, 0, 0), z1 = __builtin_amdgcn_raw_buffer_load_b128(ru, go, 0, 0),
                    z2 = __builtin_amdgcn_raw_buffer_load_b128(rd, dn, 0, 0);
        pf0 = z0.x; pf1 = z1.x; pf2 = z2.x;
      }
      SB();
    }
    if constexpr (t == 27) { if (w == 0) pst = __builtin_amdgcn_raw_buffer_load_b128(rs, psto, 0, AUX_SC1); SB(); }
  });
  asm volatile("" ::"v"(pf0), "v"(pf1), "v"(pf2));
  STAMP(5);
  {
    const float y0 = sw32(oa[0].x, oa[2].x), y1 = sw32(oa[0].y, oa[2].y), y2 = sw32(oa[1].x, oa[3].x), y3 = sw32(oa[1].y, oa[3].y);
    const float z0 = sw16(y0, y2), z1 = sw16(y1, y3);
    const u32 hi = cvtpk(z0, z1), lo = cvtpk(z0 - lo2f(hi), z1 - hi2f(hi));
    unsigned char* ob = lds + L_OB + (2 * w) * 272 + c * 16 + g * 4;
    *(u32*)ob = hi; *(u32*)(ob + 272) = lo;
  }
  if (w == 0) {
    for (u32 spin = 0; !__all(pst.y == tag && pst.w == tag) && spin < SPIN_CAP; ++spin) {
      asm volatile("" ::: "memory");
      pst = __builtin_amdgcn_raw_buffer_load_b128(rs, psto, 0, AUX_SC1);
    }
    const float m = asf(pst.x);
    float Mp = fmaxf(m, dpp<XOR1>(m)); Mp = fmaxf(Mp, dpp<XOR2>(Mp)); Mp = fmaxf(Mp, dpp<XOR7>(Mp));
    float Lp = asf(pst.z) * __builtin_amdgcn_exp2f((m - Mp) * LOG2E);
    Lp += dpp<XOR1>(Lp); Lp += dpp<XOR2>(Lp); Lp += dpp<XOR7>(Lp);
    if (lane == 0) *(f32x2*)(lds + L_MW + 64) = f32x2{Mp, Lp};
  }
  __syncthreads();
  {
    u32x4 af[4];
#pragma unroll
    for (int ks = 0; ks < 4; ++ks) af[ks] = *(const u32x4*)(lds + L_OB + c * 272 + (32 * ks + 8 * g) * 2);
    const float mk = *(const float*)(lds + L_MW + (lane & 7) * 4), lk = *(const float*)(lds + L_MW + 32 + (lane & 7) * 4);
    const f32x2 mg = *(const f32x2*)(lds + L_MW + 8 * g), pl = *(const f32x2*)(lds + L_MW + 64);
    f32x4 d[4];
#pragma unroll
    for (int e = 0; e < 4; ++e) d[e] = f32x4{0.f, 0.f, 0.f, 0.f};
#pragma unroll
    for (int ks = 0; ks < 4; ++ks)
#pragma unroll
      for (int e = 0; e < 4; ++e) d[e] = mfma16(af[ks], BO[ks * 4 + e], d[e]);
    float M = fmaxf(mk, pl.x); M = fmaxf(M, dpp<XOR1>(M)); M = fmaxf(M, dpp<XOR2>(M)); M = fmaxf(M, dpp<XOR7>(M));
    float L = lk * __builtin_amdgcn_exp2f((mk - M) * LOG2E);
    L += dpp<XOR1>(L); L += dpp<XOR2>(L); L += dpp<XOR7>(L);
    L = fmaf(pl.y, __builtin_amdgcn_exp2f((pl.x - M) * LOG2E), L);
    const float rl = __builtin_amdgcn_rcpf(L);
    const float s0 = __builtin_amdgcn_exp2f((mg.x - M) * LOG2E) * rl, s1 = __builtin_amdgcn_exp2f((mg.y - M) * LOG2E) * rl;
    float y[4];
#pragma unroll
    for (int e = 0; e < 4; ++e) y[e] = fmaf(s0, d[e].x + d[e].y, s1 * (d[e].z + d[e].w));
    float u0 = sw32(y[0], y[2]), u1 = sw32(y[1], y[3]);
    u0 = sw16(u0, u0); u1 = sw16(u1, u1);
    const u32 off = WS_Y + (u32)(((b * 64 + 8 * w + (c >> 1)) * 64 + pp) * 64 + ((c & 1) * 4 + g) * 8);
    if (!(g & 1)) __builtin_amdgcn_raw_buffer_store_b128(u32x4{asu(u0), tag, asu(u1), tag}, rs, off, 0, AUX_SC1);
  }
  STAMP(6);
  SB();
  if (is_ffn) {
    u32 WG[16], WU[16];
    u32x2 WD[8];
    {
      const __amdgpu_buffer_rsrc_t rg = mkrs(wg + (size_t)(64 * w) * FF + 32 * fj, 64 * FF * 2), ru = mkrs(wu + (size_t)(64 * w) * FF + 32 * fj, 64 * FF * 2);
      const __amdgpu_buffer_rsrc_t rd = mkrs(wd + (size_t)(32 * fj) * 512 + 64 * w, 32 * 1024);
      const u32 vo = (u32)(8 * g * FF * 2 + 4 * c), vd = (u32)(8 * g * 1024 + 8 * c);
#pragma unroll
      for (int i = 0; i < 16; ++i) {
        WG[i] = __builtin_amdgcn_raw_buffer_load_b32(rg, vo, ((i >> 3) * 32 + (i & 7)) * FF * 2, FAUX); SB();
        WU[i] = __builtin_amdgcn_raw_buffer_load_b32(ru, vo, ((i >> 3) * 32 + (i & 7)) * FF * 2, FAUX); SB();
      }
#pragma unroll
      for (int i = 0; i < 8; ++i) { WD[i] = __builtin_amdgcn_raw_buffer_load_b64(rd, vd + (i & 3) * 1024, (i >> 2) * 4096, FAUX); SB(); }
    }
    u32x4 BG[4], BU[4], BD[4];
#pragma unroll
    for (int ks = 0; ks < 2; ++ks)
#pragma unroll
      for (int e = 0; e < 2; ++e)
#pragma unroll
        for (int jj = 0; jj < 4; ++jj) {
          const u32 g0 = WG[ks * 8 + 2 * jj], g1 = WG[ks * 8 + 2 * jj + 1], u0 = WU[ks * 8 + 2 * jj], u1 = WU[ks * 8 + 2 * jj + 1];
          BG[ks * 2 + e][jj] = e ? phi(g0, g1) : plo(g0, g1);
          BU[ks * 2 + e][jj] = e ? phi(u0, u1) : plo(u0, u1);
        }
#pragma unroll
    for (int e = 0; e < 4; ++e)
#pragma unroll
      for (int jj = 0; jj < 4; ++jj) {
        const u32 r0 = WD[2 * jj][e >> 1], r1 = WD[2 * jj + 1][e >> 1];
        BD[e][jj] = (e & 1) ? phi(r0, r1) : plo(r0, r1);
      }
#pragma unroll
    for (int i = 0; i < 4; ++i) asm volatile("" : "+v"(BG[i]), "+v"(BU[i]), "+v"(BD[i]));
    SB();
    STAMP(8);
    if (w < 4) {
      const u32 ho = WS_HG + (u32)(xg * 8192 + (w * 256 + 4 * lane) * 8);
      u32x4 a0 = {0u, 0u, 0u, 0u}, a1 = {0u, 0u, 0u, 0u};
      bool k0 = false, k1 = false;
      for (u32 spin = 0;; ++spin) {
        asm volatile("" ::: "memory");
        if (!k0) a0 = __builtin_amdgcn_raw_buffer_load_b128(rs, ho, 0, AUX_SC1);
        if (!k1) a1 = __builtin_amdgcn_raw_buffer_load_b128(rs, ho + 16, 0, AUX_SC1);
        k0 = a0.y == tag && a0.w == tag; k1 = a1.y == tag && a1.w == tag;
        if (__all(k0 && k1) || spin > SPIN_CAP) break;
      }
      STAMP(7);
      const float v[8] = {lo2f(a0.x), hi2f(a0.x), lo2f(a0.z), hi2f(a0.z), lo2f(a1.x), hi2f(a1.x), lo2f(a1.z), hi2f(a1.z)};
      *(u32x4*)(lds + L_XB + w * 1040 + lane * 16) = ln512(v);
    }
    __syncthreads();
    {
      f32x4 dg[2] = {{0.f, 0.f, 0.f, 0.f}, {0.f, 0.f, 0.f, 0.f}}, du[2] = {{0.f, 0.f, 0.f, 0.f}, {0.f, 0.f, 0.f, 0.f}};
#pragma unroll
      for (int ks = 0; ks < 2; ++ks) {
        const u32x4 af = *(const u32x4*)(lds + L_XB + (c & 3) * 1040 + (64 * w + 32 * ks + 8 * g) * 2);
#pragma unroll
        for (int e = 0; e < 2; ++e) { dg[e] = mfma16(af, BG[ks * 2 + e], dg[e]); du[e] = mfma16(af, BU[ks * 2 + e], du[e]); }
      }
      if (g == 0) {
        f32x4* pt = (f32x4*)(lds + L_PT + w * 1024 + c * 16);
        pt[0] = dg[0]; pt[16] = dg[1]; pt[32] = du[0]; pt[48] = du[1];
      }
    }
    __syncthreads();
    if (w < 2) {
      const int i = lane & 15, bb = lane >> 4, eo = w;
      const float* pt = (const float*)(lds + L_PT + eo * 256 + i * 16 + bb * 4);
      float gs = 0.f, us = 0.f;
#pragma unroll
      for (int ww = 0; ww < 8; ++ww) { gs += pt[ww * 256]; us += pt[ww * 256 + 128]; }
      gs = rb(gs); us = rb(us);
      const float si = rb(gs * __builtin_amdgcn_rcpf(1.f + __builtin_amdgcn_exp2f(gs * -LOG2E)));
      *(u16*)(lds + L_AC + bb * 80 + (2 * i + eo) * 2) = (u16)(cvtpk(si * us, 0.f) & 0xffffu);
    }
    __syncthreads();
    {
      const u32x4 af = *(const u32x4*)(lds + L_AC + (c & 3) * 80 + g * 16);
      f32x4 d[4];
#pragma unroll
      for (int e = 0; e < 4; ++e) d[e] = mfma16(af, BD[e], f32x4{0.f, 0.f, 0.f, 0.f});
      const f32x4 da = (g & 1) ? d[2] : d[0], db = (g & 1) ? d[3] : d[1];
      const float a0 = (g & 2) ? da[1] : da[0], b0 = (g & 2) ? db[1] : db[0], a1 = (g & 2) ? da[3] : da[2], b1 = (g & 2) ? db[3] : db[2];
      const u32 off = WS_PO + (u32)((((g >> 1) * 64 + 8 * w + (c >> 1)) * 64 + bid) * 64 + (c & 1) * 32 + (g & 1) * 16);
      __builtin_amdgcn_raw_buffer_store_b128(u32x4{asu(a0), tag, asu(b0), tag}, rs, off, 0, AUX_SC1);
      __builtin_amdgcn_raw_buffer_store_b128(u32x4{asu(a1), tag, asu(b1), tag}, rs, off + 2u * 64u * 64u * 64u, 0, AUX_SC1);
    }
    STAMP(9);
  } else if (is_red) {
    float hval;
    {
      const u32 yo = WS_Y + (u32)((rb_ * 64 + rcs) * 4096 + lane * 16);
      u32x4 a[4];
      const u32 xres = x[rb_ * 512 + 8 * rcs + 2 * (lane & 3) + (lane >> 5)];
      bool ka[4] = {false, false, false, false};
      for (u32 spin = 0;; ++spin) {
        asm volatile("" ::: "memory");
#pragma unroll
        for (int k = 0; k < 4; ++k)
          if (!ka[k]) a[k] = __builtin_amdgcn_raw_buffer_load_b128(rs, yo + k * 1024, 0, AUX_SC1);
        int miss = 0;
#pragma unroll
        for (int k = 0; k < 4; ++k) { ka[k] = a[k].y == tag && a[k].w == tag; miss += __builtin_popcountll(__ballot(!ka[k])); }
        if (miss == 0 || spin > SPIN_CAP) break;
        if (miss > BK1) __builtin_amdgcn_s_sleep(8);
      }
      STAMP(7);
      const float v0 = (asf(a[0].x) + asf(a[1].x)) + (asf(a[2].x) + asf(a[3].x)), v1 = (asf(a[0].z) + asf(a[1].z)) + (asf(a[2].z) + asf(a[3].z));
      const float attn = reduce2q(v0, v1);
      hval = rb(lo2f(xres) + rb(attn));
      auto hs = __builtin_amdgcn_permlane32_swap(asu(hval), asu(hval), false, false);
      const u32 hp = cvtpk(hval, asf(hs[1]));
      const u32 hq = (u32)__builtin_amdgcn_mov_dpp((int)hp, XOR1, 0xf, 0xf, true);
      if (lane < 32 && !(lane & 1))
        __builtin_amdgcn_raw_buffer_store_b128(u32x4{hp, tag, hq, tag}, rs, WS_HG + (u32)((lane >> 2) * 8192 + (rb_ * 256 + 4 * rcs + (lane & 3)) * 8), 0, AUX_SC1);
      STAMP(8);
    }
    {
      const u32 po = WS_PO + (u32)((rb_ * 64 + rcs) * 4096 + lane * 16);
      u32x4 a[3];
      a[2] = u32x4{0u, 0u, 0u, 0u};
      const bool dead2 = 32 + (lane >> 2) >= JF;
      bool ka[3] = {false, false, dead2};
      for (u32 spin = 0;; ++spin) {
        asm volatile("" ::: "memory");
#pragma unroll
        for (int k = 0; k < 3; ++k)
          if (!ka[k]) a[k] = __builtin_amdgcn_raw_buffer_load_b128(rs, po + k * 1024, 0, AUX_SC1);
        ka[0] = a[0].y == tag && a[0].w == tag; ka[1] = a[1].y == tag && a[1].w == tag; ka[2] = dead2 || (a[2].y == tag && a[2].w == tag);
        const int miss = __builtin_popcountll(__ballot(!ka[0])) + __builtin_popcountll(__ballot(!ka[1])) + __builtin_popcountll(__ballot(!ka[2]));
        if (miss == 0 || spin > SPIN_CAP) break;
        if (miss > BK3) __builtin_amdgcn_s_sleep(8);
      }
      STAMP(10);
      const float v0 = (asf(a[0].x) + asf(a[1].x)) + asf(a[2].x), v1 = (asf(a[0].z) + asf(a[1].z)) + asf(a[2].z);
      const float f = reduce2q(v0, v1);
      const float o = hval + rb(f);
      auto os = __builtin_amdgcn_permlane32_swap(asu(o), asu(o), false, false);
      if (lane < 4) *(GAS u32*)(out + rb_ * 512 + 8 * rcs + 2 * lane) = cvtpk(o, asf(os[1]));
      if (task == 0 && lane == 0) __hip_atomic_store((GAS u32*)(ws + WS_EPOCH), tag, RLX_AGENT);
      STAMP(11);
    }
  }
}
}

extern "C" void kernel_launch(void* const* d_in, const int* in_sizes, int n_in, void* d_out, int out_size, void* d_ws, size_t ws_size,
                              hipStream_t stream) {
  using namespace fk;
  fused_block<<<NWG, NTHR, 0, stream>>>((const u16*)d_in[0], (const u16*)d_in[1], (const u16*)d_in[2], (const u16*)d_in[3],
                                        (const u16*)d_in[4], (const u16*)d_in[6], (const u16*)d_in[7], (const u16*)d_in[8],
                                        (u16*)d_out, (unsigned char*)d_ws);
}
```

```cpp
#include <hip/hip_runtime.h>
#include <hip/hip_bf16.h>
#include <stdint.h>
#include <type_traits>

namespace fk {
typedef unsigned short u16;
typedef unsigned u32;
typedef unsigned long long u64;
typedef u32 u32x2 __attribute__((ext_vector_type(2)));
typedef u32 u32x4 __attribute__((ext_vector_type(4)));
typedef float f32x2 __attribute__((ext_vector_type(2)));
typedef float f32x4 __attribute__((ext_vector_type(4)));
typedef short s16x8 __attribute__((ext_vector_type(8)));
typedef __bf16 bf16x2 __attribute__((ext_vector_type(2)));
#define GAS __attribute__((address_space(1)))
#define RLX_AGENT __ATOMIC_RELAXED, __HIP_MEMORY_SCOPE_AGENT

constexpr int NWG = 256, NTHR = 512, JF = 43;
constexpr int RD = 24;
constexpr int DE = 6;
constexpr int TS = 40;
constexpr int SYNCE = 8;
constexpr int SL0 = 24;
constexpr int SK = 8192, WIN = 2048, FF = 1376;
constexpr u32 WS_EPOCH = 0, WS_ST = 32768, WS_HG = 65536, WS_Y = 131072, WS_PO = WS_Y + (1u << 20), WS_BYTES = WS_PO + (1u << 20);
constexpr int AUX_SC1 = 16, AUX_NT = 2;
constexpr u32 SPIN_CAP = 1u << 18;
constexpr int L_XN = 0;
constexpr int L_QP = 8192;
constexpr int L_OB = L_QP + 4096;
constexpr int L_MW = L_OB + 4352;
constexpr int L_TAG = L_MW + 80;
constexpr int L_XB = L_TAG + 16;
constexpr int L_PT = L_XB + 4160;
constexpr int L_AC = L_PT + 8192;
constexpr int L_BYTES = L_AC + 320;

constexpr int cstep(int n) { return n < RD ? -1 : (n <= TS ? n - RD : (TS - RD) + ((n - TS) * ((63 - DE) - (TS - RD)) + (63 - TS) / 2) / (63 - TS)); }
template <int I, int N, class F> __device__ __forceinline__ void sfor(F&& f) {
  if constexpr (I < N) { f(std::integral_constant<int, I>{}); sfor<I + 1, N>(f); }
}
__device__ __forceinline__ float asf(u32 x) { return __builtin_bit_cast(float, x); }
__device__ __forceinline__ u32 asu(float x) { return __builtin_bit_cast(u32, x); }
__device__ __forceinline__ float lo2f(u32 x) { return asf(x << 16); }
__device__ __forceinline__ float hi2f(u32 x) { return asf(x & 0xffff0000u); }
__device__ __forceinline__ u32 cvtpk(float lo, float hi) {
  return __builtin_bit_cast(u32, __builtin_convertvector(f32x2{lo, hi}, bf16x2));
}
__device__ __forceinline__ float rb(float x) { return lo2f(cvtpk(x, x)); }
__device__ __forceinline__ float dot2(u32 k, u32 q, float acc) {
  return __builtin_amdgcn_fdot2_f32_bf16(__builtin_bit_cast(bf16x2, k), __builtin_bit_cast(bf16x2, q), acc, false);
}
template <int CTRL> __device__ __forceinline__ float dpp(float x) {
  return asf((u32)__builtin_amdgcn_mov_dpp((int)asu(x), CTRL, 0xf, 0xf, true));
}
constexpr int XOR1 = 0xB1, XOR2 = 0x4E, XOR7 = 0x141, XOR8 = 0x128;
__device__ __forceinline__ float xrow_sum(float x) {
  auto s = __builtin_amdgcn_permlane16_swap(asu(x), asu(x), false, false);
  x = asf(s[0]) + asf(s[1]);
  auto t = __builtin_amdgcn_permlane32_swap(asu(x), asu(x), false, false);
  return asf(t[0]) + asf(t[1]);
}
__device__ __forceinline__ float xrow_max(float x) {
  auto s = __builtin_amdgcn_permlane16_swap(asu(x), asu(x), false, false);
  x = fmaxf(asf(s[0]), asf(s[1]));
  auto t = __builtin_amdgcn_permlane32_swap(asu(x), asu(x), false, false);
  return fmaxf(asf(t[0]), asf(t[1]));
}
__device__ __forceinline__ float wave_sum(float x) {
  x += dpp<XOR1>(x); x += dpp<XOR2>(x); x += dpp<XOR7>(x); x += dpp<XOR8>(x);
  return xrow_sum(x);
}
__device__ __forceinline__ float wave_max(float x) {
  x = fmaxf(x, dpp<XOR1>(x)); x = fmaxf(x, dpp<XOR2>(x)); x = fmaxf(x, dpp<XOR7>(x)); x = fmaxf(x, dpp<XOR8>(x));
  return xrow_max(x);
}
__device__ __forceinline__ f32x4 mfma16(u32x4 a, u32x4 b, f32x4 c) {
  return __builtin_amdgcn_mfma_f32_16x16x32_bf16(__builtin_bit_cast(s16x8, a), __builtin_bit_cast(s16x8, b), c, 0, 0, 0);
}
__device__ __forceinline__ u32 plo(u32 r0, u32 r1) { return __builtin_amdgcn_perm(r1, r0, 0x05040100u); }
__device__ __forceinline__ u32 phi(u32 r0, u32 r1) { return __builtin_amdgcn_perm(r1, r0, 0x07060302u); }
__device__ __forceinline__ u32x4 ld16(const u16* p) { return *(const GAS u32x4*)p; }
#define SB() __builtin_amdgcn_sched_barrier(0)
__device__ __forceinline__ __amdgpu_buffer_rsrc_t mkrs(const void* p, int bytes) {
  return __builtin_amdgcn_make_buffer_rsrc((void*)p, 0, bytes, 0x00020000);
}

__device__ __forceinline__ float sw32(float a, float b) {
  auto s = __builtin_amdgcn_permlane32_swap(asu(a), asu(b), false, false);
  return asf(s[0]) + asf(s[1]);
}
__device__ __forceinline__ float sw16(float a, float b) {
  auto s = __builtin_amdgcn_permlane16_swap(asu(a), asu(b), false, false);
  return asf(s[0]) + asf(s[1]);
}
__device__ __forceinline__ u32x4 ln512(const float (&v)[8]) {
  const float piv = asf((u32)__builtin_amdgcn_readfirstlane((int)asu(v[0])));
  float s = 0.f, q = 0.f;
#pragma unroll
  for (int i = 0; i < 8; ++i) { const float d = v[i] - piv; s += d; q = fmaf(d, d, q); }
  s += dpp<XOR1>(s); q += dpp<XOR1>(q);
  s += dpp<XOR2>(s); q += dpp<XOR2>(q);
  s += dpp<XOR7>(s); q += dpp<XOR7>(q);
  s += dpp<XOR8>(s); q += dpp<XOR8>(q);
  float t = sw32(s, q);
  t = sw16(t, t);
  const float S = asf((u32)__builtin_amdgcn_readlane((int)asu(t), 0)), Q = asf((u32)__builtin_amdgcn_readlane((int)asu(t), 32));
  const float dm = S * (1.f / 512.f);
  const float mean = piv + dm, var = fmaxf(fmaf(-dm, dm, Q * (1.f / 512.f)), 0.f);
  const float m = rb(mean), sd = rb(__builtin_amdgcn_sqrtf(rb(rb(var) + 1e-5f)));
  const float rs = __builtin_amdgcn_rcpf(sd);
  u32x4 o;
#pragma unroll
  for (int i = 0; i < 4; ++i) { const u32 d = cvtpk(v[2 * i] - m, v[2 * i + 1] - m); o[i] = cvtpk(lo2f(d) * rs, hi2f(d) * rs); }
  return o;
}
__device__ __forceinline__ float reduce2q(float v0, float v1) {
  float t = sw32(v0, v1);
  t = sw16(t, t);
  t += dpp<0x124>(t);
  t += dpp<XOR8>(t);
  return t;
}

__global__ void __launch_bounds__(NTHR, 2)
fused_block(const u16* __restrict__ x, const u16* __restrict__ kc, const u16* __restrict__ vc, const u16* __restrict__ wq,
            const u16* __restrict__ wo, const u16* __restrict__ wg, const u16* __restrict__ wu, const u16* __restrict__ wd,
            u16* __restrict__ out, unsigned char* __restrict__ ws) {
  __shared__ __attribute__((aligned(16))) unsigned char lds[L_BYTES];
  const int tid = threadIdx.x, lane = tid & 63, w = __builtin_amdgcn_readfirstlane(tid >> 6), g = lane >> 4, c = lane & 15;
  const int bid = blockIdx.x;
  const int h = (bid & 7) + 8 * ((bid >> 3) & 3), bs = bid >> 5, b = bs >> 1, s = bs & 1, pp = 2 * h + s;
  const bool is_ffn = bid < JF;
  const int xg = bid & 7, fstart = xg < 3 ? 6 * xg : 5 * xg + 3, fcnt = xg < 3 ? 6 : 5;
  const int fj = fstart + (bid >> 3);
  const bool is_red = !is_ffn && (w == 7 || (w == 6 && bid < 2 * JF));
  const int task = bid - JF + (w == 6 ? NWG - JF : 0);
  const int rb_ = task >> 6, rcs = task & 63;
  const __amdgpu_buffer_rsrc_t rs = mkrs(ws, (int)WS_BYTES);
  const float LOG2E = 1.4426950408889634f;
  u32 epoch = 0;
  if (w == 0) epoch = __hip_atomic_load((GAS u32*)(ws + WS_EPOCH), RLX_AGENT);
  const u32x4 xr = ld16(x + b * 512 + 8 * lane);
  SB();
  u32x4 A[16];
  const __amdgpu_buffer_rsrc_t rq = mkrs(wq + (size_t)(64 * w) * 4096 + h * 128, 64 * 8192);
  const u32 voq = (u32)(16 * g * 8192 + 16 * c);
  const size_t kvoff = ((size_t)(b * 32 + h) * SK + (SK - WIN) + s * 1024 + w * 4) * 128;
  const __amdgpu_buffer_rsrc_t rk = mkrs(kc + kvoff, 262144 - 7168), rv = mkrs(vc + kvoff, 262144 - 7168);
  const u32 lo16 = (u32)lane * 16u;
#define LDK(t) __builtin_amdgcn_raw_buffer_load_b128(rk, lo16, (t) * 8192, AUX_NT)
#define LDV(t) __builtin_amdgcn_raw_buffer_load_b128(rv, lo16, (t) * 8192, AUX_NT)
#define LDN(n) ((n) < 32 ? LDK(n) : LDV((n) - 32))
  u32x4 R[RD];
#pragma unroll
  for (int t = 0; t < RD; ++t) {
    if (t < 16) { A[t] = __builtin_amdgcn_raw_buffer_load_b128(rq, voq, t * 8192, AUX_NT); SB(); }
    R[t] = LDN(t); SB();
  }
  if (tid == 0) *(u32*)(lds + L_TAG) = epoch;
  {
    const float v[8] = {lo2f(xr[0]), hi2f(xr[0]), lo2f(xr[1]), hi2f(xr[1]), lo2f(xr[2]), hi2f(xr[2]), lo2f(xr[3]), hi2f(xr[3])};
    *(u32x4*)(lds + L_XN + w * 1024 + lane * 16) = ln512(v);
  }
  u32x4 xa, xb;
  {
    const unsigned char* p = lds + L_XN + w * 1024 + (64 * w + 16 * g) * 2;
    __builtin_amdgcn_fence(__ATOMIC_RELEASE, "wavefront");
    xa = *(const u32x4*)p; xb = *(const u32x4*)(p + 16);
  }
  {
    f32x2 qa[4] = {{0.f, 0.f}, {0.f, 0.f}, {0.f, 0.f}, {0.f, 0.f}};
#pragma unroll
    for (int t = 0; t < 16; ++t) {
      const u32 pk = t < 8 ? xa[(t >> 1) & 3] : xb[(t >> 1) & 3];
      const float xv = (t & 1) ? hi2f(pk) : lo2f(pk);
#pragma unroll
      for (int k = 0; k < 4; ++k) qa[k] = __builtin_elementwise_fma(f32x2{lo2f(A[t][k]), hi2f(A[t][k])}, f32x2{xv, xv}, qa[k]);
    }
    const float y0 = sw32(qa[0].x, qa[2].x), y1 = sw32(qa[0].y, qa[2].y), y2 = sw32(qa[1].x, qa[3].x), y3 = sw32(qa[1].y, qa[3].y);
    *(f32x2*)(lds + L_QP + w * 512 + c * 32 + g * 8) = f32x2{sw16(y0, y2), sw16(y1, y3)};
  }
  asm volatile("" ::: "memory");
  SB();
  u32x2 WO[32];
  const __amdgpu_buffer_rsrc_t ro = mkrs(wo + (size_t)(h * 128) * 512 + 64 * w, 128 * 1024);
  const u32 voo = (u32)(8 * g * 1024 + 8 * c);
#define LDO(i) __builtin_amdgcn_raw_buffer_load_b64(ro, voo + ((i) & 3) * 1024, ((i) >> 3) * 32768 + (((i) >> 2) & 1) * 4096, AUX_NT)
#pragma unroll
  for (int i = 0; i < 8; ++i) { WO[i] = LDO(i); SB(); }
  __syncthreads();
  const u32 tag = *(const u32*)(lds + L_TAG) + 1u;
  u32x4 qf;
  {
    const f32x4* p0 = (const f32x4*)(lds + L_QP + (2 * g) * 512 + c * 32);
    const f32x4* p1 = (const f32x4*)(lds + L_QP + (2 * g + 1) * 512 + c * 32);
    const f32x4 a0 = p0[0] + p1[0], a1 = p0[1] + p1[1];
    qf = u32x4{cvtpk(xrow_sum(a0.x), xrow_sum(a0.y)), cvtpk(xrow_sum(a0.z), xrow_sum(a0.w)),
               cvtpk(xrow_sum(a1.x), xrow_sum(a1.y)), cvtpk(xrow_sum(a1.z), xrow_sum(a1.w))};
  }
  float sc[2];
#pragma unroll
  for (int G = 0; G < 2; ++G) {
    float xs[16];
#pragma unroll
    for (int t = 0; t < 16; ++t) {
      float d = 0.f;
#pragma unroll
      for (int k = 0; k < 4; ++k) d = dot2(R[(16 * G + t) % RD][k], qf[k], d);
      asm volatile("" : "+v"(d)::"memory");
      xs[t] = d;
      SB();
#pragma unroll
      for (int n = RD; n < 64; ++n) if (cstep(n) == 16 * G + t) { R[n % RD] = LDN(n); SB(); }
      if (16 * G + t < 24) { WO[8 + 16 * G + t] = LDO(8 + 16 * G + t); SB(); }
      if ((16 * G + t + 1) % SYNCE == 0) { __builtin_amdgcn_s_barrier(); SB(); }
    }
#define TR_STEP(HALF, CTRL, BIT) _Pragma("unroll") for (int hh = 0; hh < (HALF); ++hh) { \
      const float t1_ = xs[hh] + dpp<CTRL>(xs[hh]), t2_ = xs[hh + (HALF)] + dpp<CTRL>(xs[hh + (HALF)]); xs[hh] = (c & (BIT)) ? t2_ : t1_; }
    TR_STEP(8, XOR8, 8) TR_STEP(4, XOR7, 4) TR_STEP(2, XOR2, 2) TR_STEP(1, XOR1, 1)
#undef TR_STEP
    sc[G] = rb(rb(xs[0]) * 0.08838834764831845f);
    asm volatile("" : "+v"(sc[G])::"memory");
    SB();
  }
  const float mw = wave_max(fmaxf(sc[0], sc[1]));
  float p[2];
  p[0] = __builtin_amdgcn_exp2f((sc[0] - mw) * LOG2E);
  p[1] = __builtin_amdgcn_exp2f((sc[1] - mw) * LOG2E);
  const float lw = wave_sum(p[0] + p[1]);
  if (lane == 0) {
    __builtin_amdgcn_raw_buffer_store_b128(u32x4{asu(mw), tag, asu(lw), tag}, rs, WS_ST + (u32)(((b * 64 + pp) * 8 + w) * 16), 0, AUX_SC1);
    *(float*)(lds + L_MW + w * 4) = mw; *(float*)(lds + L_MW + 32 + w * 4) = lw;
  }
  SB();
  f32x2 oa[4] = {{0.f, 0.f}, {0.f, 0.f}, {0.f, 0.f}, {0.f, 0.f}};
  u32x4 BO[16];
  u32x4 pst = {0u, 0u, 0u, 0u};
  u32 pf0 = 0;
  const u32 psto = WS_ST + (u32)(((b * 64 + (pp ^ 1)) * 8 + (lane & 7)) * 16);
  sfor<0, 32>([&](auto I) {
    constexpr int t = I;
    if constexpr (t == 24) {
#pragma unroll
      for (int ks = 0; ks < 4; ++ks)
#pragma unroll
        for (int e = 0; e < 4; ++e)
#pragma unroll
          for (int jj = 0; jj < 4; ++jj) {
            const u32 r0 = WO[ks * 8 + 2 * jj][e >> 1], r1 = WO[ks * 8 + 2 * jj + 1][e >> 1];
            BO[ks * 4 + e][jj] = (e & 1) ? phi(r0, r1) : plo(r0, r1);
          }
#pragma unroll
      for (int i = 0; i < 16; ++i) asm volatile("" : "+v"(BO[i]));
      SB();
    }
    const float pb = asf((u32)__builtin_amdgcn_update_dpp(0, (int)asu(p[t >> 4]), 0x150 + (t & 15), 0xf, 0xf, true));
    const u32x4 v = R[(32 + t) % RD];
#pragma unroll
    for (int k = 0; k < 4; ++k) oa[k] = __builtin_elementwise_fma(f32x2{lo2f(v[k]), hi2f(v[k])}, f32x2{pb, pb}, oa[k]);
    asm volatile("" : "+v"(oa[0]), "+v"(oa[1]), "+v"(oa[2]), "+v"(oa[3])::"memory");
    SB();
#pragma unroll
    for (int n = RD; n < 64; ++n) if (cstep(n) == 32 + t) { R[n % RD] = LDN(n); SB(); }
    if constexpr ((t + 1) % SYNCE == 0 && t < 31) { __builtin_amdgcn_s_barrier(); SB(); }
    if constexpr (t == 27) { if (w == 0) pst = __builtin_amdgcn_raw_buffer_load_b128(rs, psto, 0, AUX_SC1); SB(); }
  });
  {
    const float y0 = sw32(oa[0].x, oa[2].x), y1 = sw32(oa[0].y, oa[2].y), y2 = sw32(oa[1].x, oa[3].x), y3 = sw32(oa[1].y, oa[3].y);
    const float z0 = sw16(y0, y2), z1 = sw16(y1, y3);
    const u32 hi = cvtpk(z0, z1), lo = cvtpk(z0 - lo2f(hi), z1 - hi2f(hi));
    unsigned char* ob = lds + L_OB + (2 * w) * 272 + c * 16 + g * 4;
    *(u32*)ob = hi; *(u32*)(ob + 272) = lo;
  }
  if (w == 0) {
    for (u32 spin = 0; !__all(pst.y == tag && pst.w == tag) && spin < SPIN_CAP; ++spin) {
      asm volatile("" ::: "memory");
      pst = __builtin_amdgcn_raw_buffer_load_b128(rs, psto, 0, AUX_SC1);
    }
    const float m = asf(pst.x);
    float Mp = fmaxf(m, dpp<XOR1>(m)); Mp = fmaxf(Mp, dpp<XOR2>(Mp)); Mp = fmaxf(Mp, dpp<XOR7>(Mp));
    float Lp = asf(pst.z) * __builtin_amdgcn_exp2f((m - Mp) * LOG2E);
    Lp += dpp<XOR1>(Lp); Lp += dpp<XOR2>(Lp); Lp += dpp<XOR7>(Lp);
    if (lane == 0) *(f32x2*)(lds + L_MW + 64) = f32x2{Mp, Lp};
  }
  __syncthreads();
  {
    u32x4 af[4];
#pragma unroll
    for (int ks = 0; ks < 4; ++ks) af[ks] = *(const u32x4*)(lds + L_OB + c * 272 + (32 * ks + 8 * g) * 2);
    const float mk = *(const float*)(lds + L_MW + (lane & 7) * 4), lk = *(const float*)(lds + L_MW + 32 + (lane & 7) * 4);
    const f32x2 mg = *(const f32x2*)(lds + L_MW + 8 * g), pl = *(const f32x2*)(lds + L_MW + 64);
    f32x4 d[4];
#pragma unroll
    for (int e = 0; e < 4; ++e) d[e] = f32x4{0.f, 0.f, 0.f, 0.f};
#pragma unroll
    for (int ks = 0; ks < 4; ++ks)
#pragma unroll
      for (int e = 0; e < 4; ++e) d[e] = mfma16(af[ks], BO[ks * 4 + e], d[e]);
    float M = fmaxf(mk, pl.x); M = fmaxf(M, dpp<XOR1>(M)); M = fmaxf(M, dpp<XOR2>(M)); M = fmaxf(M, dpp<XOR7>(M));
    float L = lk * __builtin_amdgcn_exp2f((mk - M) * LOG2E);
    L += dpp<XOR1>(L); L += dpp<XOR2>(L); L += dpp<XOR7>(L);
    L = fmaf(pl.y, __builtin_amdgcn_exp2f((pl.x - M) * LOG2E), L);
    const float rl = __builtin_amdgcn_rcpf(L);
    const float s0 = __builtin_amdgcn_exp2f((mg.x - M) * LOG2E) * rl, s1 = __builtin_amdgcn_exp2f((mg.y - M) * LOG2E) * rl;
    float y[4];
#pragma unroll
    for (int e = 0; e < 4; ++e) y[e] = fmaf(s0, d[e].x + d[e].y, s1 * (d[e].z + d[e].w));
    float u0 = sw32(y[0], y[2]), u1 = sw32(y[1], y[3]);
    u0 = sw16(u0, u0); u1 = sw16(u1, u1);
    const u32 off = WS_Y + (u32)(((b * 64 + 8 * w + 2 * (c >> 2) + (g >> 1)) * 64 + pp) * 64 + (c & 3) * 16);
    if (!(g & 1)) __builtin_amdgcn_raw_buffer_store_b128(u32x4{asu(u0), tag, asu(u1), tag}, rs, off, 0, AUX_SC1);
  }
  __builtin_amdgcn_s_barrier();
  SB();
  if (!is_ffn && w <= 5) {
    const int gi = (bid >> 3) - fcnt;
    const int n = (gi * 6 + w) * 64 + lane, per = 512 * fcnt;
    const int m = n >= 2 * per ? 2 : (n >= per ? 1 : 0), r = n - m * per;
    const int row = fcnt == 6 ? r / 6 : r / 5, seg = r - row * fcnt;
    const u16* pa = m == 2 ? wd + (size_t)fstart * 32 * 512 + r * 32 : (m ? wu : wg) + (size_t)row * FF + (fstart + seg) * 32;
    if (n < 3 * per) pf0 = *(const GAS u32*)pa;
    asm volatile("" ::"v"(pf0));
  }
  if (is_ffn) {
    u32 WG[16], WU[16];
    u32x2 WD[8];
    {
      const __amdgpu_buffer_rsrc_t rg = mkrs(wg + (size_t)(64 * w) * FF + 32 * fj, 64 * FF * 2), ru = mkrs(wu + (size_t)(64 * w) * FF + 32 * fj, 64 * FF * 2);
      const __amdgpu_buffer_rsrc_t rd = mkrs(wd + (size_t)(32 * fj) * 512 + 64 * w, 32 * 1024);
      const u32 vo = (u32)(8 * g * FF * 2 + 4 * c), vd = (u32)(8 * g * 1024 + 8 * c);
#pragma unroll
      for (int i = 0; i < 16; ++i) {
        WG[i] = __builtin_amdgcn_raw_buffer_load_b32(rg, vo, ((i >> 3) * 32 + (i & 7)) * FF * 2, 0); SB();
        WU[i] = __builtin_amdgcn_raw_buffer_load_b32(ru, vo, ((i >> 3) * 32 + (i & 7)) * FF * 2, 0); SB();
      }
#pragma unroll
      for (int i = 0; i < 8; ++i) { WD[i] = __builtin_amdgcn_raw_buffer_load_b64(rd, vd + (i & 3) * 1024, (i >> 2) * 4096, 0); SB(); }
    }
    u32x4 BG[4], BU[4], BD[4];
#pragma unroll
    for (int ks = 0; ks < 2; ++ks)
#pragma unroll
      for (int e = 0; e < 2; ++e)
#pragma unroll
        for (int jj = 0; jj < 4; ++jj) {
          const u32 g0 = WG[ks * 8 + 2 * jj], g1 = WG[ks * 8 + 2 * jj + 1], u0 = WU[ks * 8 + 2 * jj], u1 = WU[ks * 8 + 2 * jj + 1];
          BG[ks * 2 + e][jj] = e ? phi(g0, g1) : plo(g0, g1);
          BU[ks * 2 + e][jj] = e ? phi(u0, u1) : plo(u0, u1);
        }
#pragma unroll
    for (int e = 0; e < 4; ++e)
#pragma unroll
      for (int jj = 0; jj < 4; ++jj) {
        const u32 r0 = WD[2 * jj][e >> 1], r1 = WD[2 * jj + 1][e >> 1];
        BD[e][jj] = (e & 1) ? phi(r0, r1) : plo(r0, r1);
      }
#pragma unroll
    for (int i = 0; i < 4; ++i) asm volatile("" : "+v"(BG[i]), "+v"(BU[i]), "+v"(BD[i]));
    SB();
    if (w < 4) {
      const u32 ho = WS_HG + (u32)(xg * 8192 + (w * 256 + 4 * lane) * 8);
      u32x4 a0 = {0u, 0u, 0u, 0u}, a1 = {0u, 0u, 0u, 0u};
      bool k0 = false, k1 = false;
      for (u32 spin = 0;; ++spin) {
        asm volatile("" ::: "memory");
        if (!k0) a0 = __builtin_amdgcn_raw_buffer_load_b128(rs, ho, 0, AUX_SC1);
        if (!k1) a1 = __builtin_amdgcn_raw_buffer_load_b128(rs, ho + 16, 0, AUX_SC1);
        k0 = a0.y == tag && a0.w == tag; k1 = a1.y == tag && a1.w == tag;
        if (__all(k0 && k1) || spin > SPIN_CAP) break;
      }
      const float v[8] = {lo2f(a0.x), hi2f(a0.x), lo2f(a0.z), hi2f(a0.z), lo2f(a1.x), hi2f(a1.x), lo2f(a1.z), hi2f(a1.z)};
      *(u32x4*)(lds + L_XB + w * 1040 + lane * 16) = ln512(v);
    }
    __syncthreads();
    {
      f32x4 dg[2] = {{0.f, 0.f, 0.f, 0.f}, {0.f, 0.f, 0.f, 0.f}}, du[2] = {{0.f, 0.f, 0.f, 0.f}, {0.f, 0.f, 0.f, 0.f}};
#pragma unroll
      for (int ks = 0; ks < 2; ++ks) {
        const u32x4 af = *(const u32x4*)(lds + L_XB + (c & 3) * 1040 + (64 * w + 32 * ks + 8 * g) * 2);
#pragma unroll
        for (int e = 0; e < 2; ++e) { dg[e] = mfma16(af, BG[ks * 2 + e], dg[e]); du[e] = mfma16(af, BU[ks * 2 + e], du[e]); }
      }
      if (g == 0) {
        f32x4* pt = (f32x4*)(lds + L_PT + w * 1024 + c * 16);
        pt[0] = dg[0]; pt[16] = dg[1]; pt[32] = du[0]; pt[48] = du[1];
      }
    }
    __syncthreads();
    if (w < 2) {
      const int i = lane & 15, bb = lane >> 4, eo = w;
      const float* pt = (const float*)(lds + L_PT + eo * 256 + i * 16 + bb * 4);
      float gs = 0.f, us = 0.f;
#pragma unroll
      for (int ww = 0; ww < 8; ++ww) { gs += pt[ww * 256]; us += pt[ww * 256 + 128]; }
      gs = rb(gs); us = rb(us);
      const float si = rb(gs * __builtin_amdgcn_rcpf(1.f + __builtin_amdgcn_exp2f(gs * -LOG2E)));
      *(u16*)(lds + L_AC + bb * 80 + (2 * i + eo) * 2) = (u16)(cvtpk(si * us, 0.f) & 0xffffu);
    }
    __syncthreads();
    {
      const u32x4 af = *(const u32x4*)(lds + L_AC + (c & 3) * 80 + g * 16);
      f32x4 d[4];
#pragma unroll
      for (int e = 0; e < 4; ++e) d[e] = mfma16(af, BD[e], f32x4{0.f, 0.f, 0.f, 0.f});
      const f32x4 da = (g & 1) ? d[2] : d[0], db = (g & 1) ? d[3] : d[1];
      const float a0 = (g & 2) ? da[1] : da[0], b0 = (g & 2) ? db[1] : db[0], a1 = (g & 2) ? da[3] : da[2], b1 = (g & 2) ? db[3] : db[2];
      const u32 off = WS_PO + (u32)((((g >> 1) * 64 + 8 * w + 2 * (c >> 2) + (g & 1)) * 64 + bid) * 64 + (c & 3) * 16);
      __builtin_amdgcn_raw_buffer_store_b128(u32x4{asu(a0), tag, asu(b0), tag}, rs, off, 0, AUX_SC1);
      __builtin_amdgcn_raw_buffer_store_b128(u32x4{asu(a1), tag, asu(b1), tag}, rs, off + 2u * 64u * 64u * 64u, 0, AUX_SC1);
    }
  } else if (is_red) {
    float hval;
    const int rcol = 16 * (rcs >> 1) + 4 * (lane & 3) + 2 * (rcs & 1);
    {
      const u32 yo = WS_Y + (u32)((rb_ * 64 + rcs) * 4096 + lane * 16);
      u32x4 a[4];
      const u32 xres = x[rb_ * 512 + rcol + (lane >> 5)];
      bool ka[4] = {false, false, false, false};
      __builtin_amdgcn_s_sleep(SL0);
      for (u32 spin = 0;; ++spin) {
        asm volatile("" ::: "memory");
#pragma unroll
        for (int k = 0; k < 4; ++k)
          if (!ka[k]) a[k] = __builtin_amdgcn_raw_buffer_load_b128(rs, yo + k * 1024, 0, AUX_SC1);
        int miss = 0;
#pragma unroll
        for (int k = 0; k < 4; ++k) { ka[k] = a[k].y == tag && a[k].w == tag; miss += __builtin_popcountll(__ballot(!ka[k])); }
        if (miss == 0 || spin > SPIN_CAP) break;
        if (miss > 24) __builtin_amdgcn_s_sleep(8);
      }
      const float v0 = (asf(a[0].x) + asf(a[1].x)) + (asf(a[2].x) + asf(a[3].x)), v1 = (asf(a[0].z) + asf(a[1].z)) + (asf(a[2].z) + asf(a[3].z));
      const float attn = reduce2q(v0, v1);
      hval = rb(lo2f(xres) + rb(attn));
      auto hs = __builtin_amdgcn_permlane32_swap(asu(hval), asu(hval), false, false);
      const u32 hp = cvtpk(hval, asf(hs[1]));
      if (lane < 32) __builtin_amdgcn_raw_buffer_store_b64(u32x2{hp, tag}, rs, WS_HG + (u32)((lane >> 2) * 8192 + (rb_ * 256 + (rcol >> 1)) * 8), 0, AUX_SC1);
    }
    {
      const u32 po = WS_PO + (u32)((rb_ * 64 + rcs) * 4096 + lane * 16);
      u32x4 a[3];
      a[2] = u32x4{0u, 0u, 0u, 0u};
      const bool dead2 = 32 + (lane >> 2) >= JF;
      bool ka[3] = {false, false, dead2};
      for (u32 spin = 0;; ++spin) {
        asm volatile("" ::: "memory");
#pragma unroll
        for (int k = 0; k < 3; ++k)
          if (!ka[k]) a[k] = __builtin_amdgcn_raw_buffer_load_b128(rs, po + k * 1024, 0, AUX_SC1);
        ka[0] = a[0].y == tag && a[0].w == tag; ka[1] = a[1].y == tag && a[1].w == tag; ka[2] = dead2 || (a[2].y == tag && a[2].w == tag);
        const int miss = __builtin_popcountll(__ballot(!ka[0])) + __builtin_popcountll(__ballot(!ka[1])) + __builtin_popcountll(__ballot(!ka[2]));
        if (miss == 0 || spin > SPIN_CAP) break;
        if (miss > 24) __builtin_amdgcn_s_sleep(8);
      }
      const float v0 = (asf(a[0].x) + asf(a[1].x)) + asf(a[2].x), v1 = (asf(a[0].z) + asf(a[1].z)) + asf(a[2].z);
      const float f = reduce2q(v0, v1);
      const float o = hval + rb(f);
      auto os = __builtin_amdgcn_permlane32_swap(asu(o), asu(o), false, false);
      if (lane < 4) *(GAS u32*)(out + rb_ * 512 + rcol) = cvtpk(o, asf(os[1]));
      if (task == 0 && lane == 0) __hip_atomic_store((GAS u32*)(ws + WS_EPOCH), tag, RLX_AGENT);
    }
  }
}
}

extern "C" void kernel_launch(void* const* d_in, const int* in_sizes, int n_in, void* d_out, int out_size, void* d_ws, size_t ws_size,
                              hipStream_t stream) {
  using namespace fk;
  fused_block<<<NWG, NTHR, 0, stream>>>((const u16*)d_in[0], (const u16*)d_in[1], (const u16*)d_in[2], (const u16*)d_in[3],
                                        (const u16*)d_in[4], (const u16*)d_in[6], (const u16*)d_in[7], (const u16*)d_in[8],
                                        (u16*)d_out, (unsigned char*)d_ws);
}
```
